```python
import torch

def _softmax(x, axis=-1):
    return torch.softmax(x, dim=axis)
def _gelu(x):
    return torch.nn.functional.gelu(x, approximate="tanh")
def _silu(x):
    return torch.nn.functional.silu(x)
def _sigmoid(x):
    return torch.sigmoid(x)
def _layernorm(x, axis=-1, eps=1e-5):
    m = x.mean(dim=axis, keepdim=True)
    v = x.var(dim=axis, keepdim=True, unbiased=False)
    return (x - m) / torch.sqrt(v + eps)
def _scatter_add(tgt, idx, src, axis=0):
    return tgt.clone().index_add_(axis, idx, src)
def _segment_sum(src, seg, n):
    out = torch.zeros((n,) + src.shape[1:], dtype=src.dtype)
    return out.index_add_(0, seg, src)
def _pad(x, pw):
    flat = tuple(v for pair in reversed(pw) for v in pair)
    return torch.nn.functional.pad(x, flat)

kernel_name = 'sequential_preLN__grouped_kv_mqa__dense_activation_squared_relu__prompt'

def setup_inputs():

    _w = (lambda a: (lambda t: t.to(torch.bfloat16) if t.is_floating_point() else t)(torch.as_tensor(a)))
    def _A_setup():
        B, S, D, H = 1, 2048, 640, 32
        Dh = D // H
        return {
            "x": _w((lambda *s: torch.randn(*s, dtype=torch.bfloat16) * 0.05)(B, S, D)),
            "wq": _w((lambda *s: torch.randn(*s, dtype=torch.bfloat16) * 0.05)(D, H * Dh)),
            "wk": _w((lambda *s: torch.randn(*s, dtype=torch.bfloat16) * 0.05)(D, Dh)),
            "wv": _w((lambda *s: torch.randn(*s, dtype=torch.bfloat16) * 0.05)(D, Dh)),
            "wo": _w((lambda *s: torch.randn(*s, dtype=torch.bfloat16) * 0.05)(H * Dh, D)),
        }
    _A_in = _A_setup()

    def _B_setup():
        B, S, D, F = 1, 2048, 640, 1536
        return {
            "x": _w((lambda *s: torch.randn(*s, dtype=torch.bfloat16) * 0.05)(B, S, D)),
            "w1": _w((lambda *s: torch.randn(*s, dtype=torch.bfloat16) * 0.05)(D, F)),
            "b1": _w((lambda *s: torch.randn(*s, dtype=torch.bfloat16) * 0.8)(F)),
            "w2": _w((lambda *s: torch.randn(*s, dtype=torch.bfloat16) * 0.05)(F, D)),
            "b2": _w((lambda *s: torch.randn(*s, dtype=torch.bfloat16) * 3.2)(D)),
        }
    _B_in = _B_setup()

    return {
        "A_x": _A_in["x"],
        "A_wq": _A_in["wq"],
        "A_wk": _A_in["wk"],
        "A_wv": _A_in["wv"],
        "A_wo": _A_in["wo"],
        "B_x": _B_in["x"],
        "B_w1": _B_in["w1"],
        "B_b1": _B_in["b1"],
        "B_w2": _B_in["w2"],
        "B_b2": _B_in["b2"],
    }

def reference(A_x, A_wq, A_wk, A_wv, A_wo, B_x, B_w1, B_b1, B_w2, B_b2):
    def _A_ref(x, wq, wk, wv, wo):
        B, S, D = x.shape
        H = 32
        Dh = D // H
        q = (lambda x, *d: x.permute(*d))((x @ wq).reshape(B, S, H, Dh), 0, 2, 1, 3)
        k = x @ wk
        v = x @ wv
        scores = torch.einsum("bhqd,bkd->bhqk", q, k) * (Dh ** -0.5)
        p = _softmax(scores, axis=-1)
        o = torch.einsum("bhqk,bkd->bhqd", p, v)
        return (lambda x, *d: x.permute(*d))(o, 0, 2, 1, 3).reshape(B, S, H * Dh) @ wo

    def _B_ref(x, w1, b1, w2, b2):
        h = torch.einsum("bsd,df->bsf", x, w1) + b1
        zero = torch.zeros_like(h)
        r = torch.where(h > zero, h, zero)
        h = r * r
        out = torch.einsum("bsf,fd->bsd", h, w2) + b2
        return out

    x = A_x
    h = x + _A_ref(_layernorm(x, axis=-1), A_wq, A_wk, A_wv, A_wo)
    h2 = h + _B_ref(_layernorm(h, axis=-1), B_w1, B_b1, B_w2, B_b2)
    return h2
```

```cpp
#include <hip/hip_runtime.h>
#include <hip/hip_bf16.h>
#include <stdint.h>

typedef short  s16x4  __attribute__((ext_vector_type(4)));
typedef __bf16 bf16x8 __attribute__((ext_vector_type(8)));
typedef __bf16 bf16x2 __attribute__((ext_vector_type(2)));
typedef float  f32x2  __attribute__((ext_vector_type(2)));
typedef float  f32x4  __attribute__((ext_vector_type(4)));
typedef float  f32x16 __attribute__((ext_vector_type(16)));
typedef unsigned u32x2 __attribute__((ext_vector_type(2)));
typedef unsigned u32x4 __attribute__((ext_vector_type(4)));
#define LAS __attribute__((address_space(3)))
#define GAS __attribute__((address_space(1)))
#define DEVI __device__ __forceinline__

constexpr int S = 2048, D = 640, H = 32, DH = 20, F = 1536;
constexpr float QSCALE = 0.22360679774997896f * 1.4426950408889634f;
constexpr float EPS = 1e-5f;

constexpr size_t WS_Q    = 0;
constexpr int    KV_TILE = 2624;
constexpr size_t WS_KV   = WS_Q + (size_t)S * D * 2;
constexpr size_t WS_KMAX = WS_KV + 64 * KV_TILE;
constexpr size_t WS_OF   = WS_KMAX + 512;
constexpr size_t WS_HF   = WS_OF + (size_t)S * D * 2;
constexpr size_t WS_ST   = WS_HF + (size_t)S * D * 2;
constexpr size_t WS_H1F  = WS_ST + (size_t)S * 8 * 2 * 4;
constexpr size_t WS_WOF  = WS_H1F + (size_t)S * F * 2;
constexpr size_t WS_W1F  = WS_WOF + (size_t)D * D * 2;
constexpr size_t WS_W2F  = WS_W1F + (size_t)D * F * 2;
constexpr size_t WS_CS1  = WS_W2F + (size_t)D * F * 2;
constexpr size_t WS_END  = WS_CS1 + (size_t)20 * F * 4;

DEVI unsigned pk_bf16(float lo, float hi) {
  f32x2 v = {lo, hi};
  bf16x2 b = __builtin_convertvector(v, bf16x2);
  return __builtin_bit_cast(unsigned, b);
}
DEVI float bf_lo(unsigned u) { return __uint_as_float(u << 16); }
DEVI float bf_hi(unsigned u) { return __uint_as_float(u & 0xffff0000u); }
DEVI bf16x8 as_frag(u32x4 v) { return __builtin_bit_cast(bf16x8, v); }
DEVI bf16x8 frag2(s16x4 a, s16x4 b) {
  u32x2 x = __builtin_bit_cast(u32x2, a), y = __builtin_bit_cast(u32x2, b);
  u32x4 v = {x[0], x[1], y[0], y[1]};
  return __builtin_bit_cast(bf16x8, v);
}
DEVI s16x4 tr_read(const char* p) { return __builtin_amdgcn_ds_read_tr16_b64_v4i16((LAS s16x4*)p); }
DEVI void glds16(const void* g, char* l) { __builtin_amdgcn_global_load_lds((const GAS void*)g, (LAS void*)l, 16, 0, 0); }
DEVI f32x4 mfma16(bf16x8 a, bf16x8 b, f32x4 c) { return __builtin_amdgcn_mfma_f32_16x16x32_bf16(a, b, c, 0, 0, 0); }
DEVI f32x16 mfma32(bf16x8 a, bf16x8 b, f32x16 c) { return __builtin_amdgcn_mfma_f32_32x32x16_bf16(a, b, c, 0, 0, 0); }
DEVI f32x16 mfma32k8(u32x2 a, u32x2 b, f32x16 c) {
  return __builtin_amdgcn_mfma_f32_32x32x8bf16_1k(__builtin_bit_cast(s16x4, a), __builtin_bit_cast(s16x4, b), c, 0, 0, 0);
}
DEVI float swap32(float x) {
  auto r = __builtin_amdgcn_permlane32_swap(__float_as_uint(x), __float_as_uint(x), false, false);
  return __uint_as_float((threadIdx.x & 32) ? r[0] : r[1]);
}
DEVI void map_cj(int b, int& c, int& j) { c = (b & 7) * 4 + (b >> 6); j = (b >> 3) & 7; }

constexpr int K1_WQ = 0;
constexpr int K1_WKV = 640 * 160;
constexpr int K1_CS = K1_WKV + 26624;
constexpr int K1_LDS = K1_CS + 512;

__global__ void __launch_bounds__(512, 2) k1_qkv(const uint16_t* __restrict__ x, const uint16_t* __restrict__ wq,
                                                 const uint16_t* __restrict__ wk, const uint16_t* __restrict__ wv,
                                                 char* __restrict__ ws) {
  extern __shared__ __attribute__((aligned(16))) char smem[];
  int c, j; map_cj(blockIdx.x, c, j);
  const int tid = threadIdx.x, lane = tid & 63, w = __builtin_amdgcn_readfirstlane(tid >> 6);
  const int i16 = lane & 15, g = lane >> 4;
  const int tg = w < 4 ? w : (j >> 1);
  u32x4 xf[20];
  if (w <= 4) {
    const uint16_t* xr = x + (size_t)(64 * c + 16 * tg + i16) * D + 8 * g;
#pragma unroll
    for (int s = 0; s < 20; s++) xf[s] = *(const u32x4*)(xr + 32 * s);
  }
  {
    const char* wkv = (const char*)((j & 1) ? wv : wk);
    for (int p = w; p < 25; p += 8) glds16(wkv + p * 1024 + lane * 16, smem + K1_WKV + p * 1024);
    for (int p = w; p < 100; p += 8) {
      int q = p * 64 + lane, slot = q / 10, ch = q - slot * 10;
      int k = (slot & ~12) | ((slot & 4) << 1) | ((slot & 8) >> 1);
      glds16((const char*)wq + (size_t)k * (D * 2) + 160 * j + 16 * ch, smem + K1_WQ + p * 1024);
    }
  }
  __syncthreads();
  u32x4 ones4 = {0x3f803f80u, 0x3f803f80u, 0x3f803f80u, 0x3f803f80u};
  const bf16x8 ones = as_frag(ones4);
  const int q4 = (lane & 15) >> 2, p4 = lane & 3;
  f32x4 acc[5] = {}, gram = {}, sum = {};
  if (w < 4 || w == 5) {
    const char* bp = smem + K1_WQ + (16 * (g >> 1) + 4 * (g & 1) + q4) * 160 + 8 * p4;
#pragma unroll
    for (int s = 0; s < 20; s++) {
      bf16x8 xb = w == 5 ? ones : as_frag(xf[s]);
#pragma unroll
      for (int n = 0; n < 5; n++) {
        bf16x8 wf = frag2(tr_read(bp + s * 32 * 160 + n * 32), tr_read(bp + s * 32 * 160 + 8 * 160 + n * 32));
        acc[n] = mfma16(wf, xb, acc[n]);
      }
      gram = mfma16(xb, xb, gram);
      sum = mfma16(ones, xb, sum);
    }
    if (w == 5 && i16 == 0) {
#pragma unroll
      for (int n = 0; n < 5; n++) *(f32x4*)(smem + K1_CS + (16 * n + 4 * g) * 4) = acc[n];
    }
  } else if (w == 4) {
    const char* bp = smem + K1_WKV + (8 * g + q4) * 40 + 8 * p4;
    f32x4 cs[2] = {};
    if ((j & 1) == 0) {
#pragma unroll
      for (int s = 0; s < 20; s++) {
        bf16x8 xb = as_frag(xf[s]);
#pragma unroll
        for (int n = 0; n < 2; n++) {
          bf16x8 wf = frag2(tr_read(bp + s * 32 * 40 + n * 32), tr_read(bp + s * 32 * 40 + 4 * 40 + n * 32));
          acc[n] = mfma16(wf, xb, acc[n]);
          cs[n] = mfma16(wf, ones, cs[n]);
        }
        gram = mfma16(xb, xb, gram);
        sum = mfma16(ones, xb, sum);
      }
      float d = p4 == 0 ? gram[0] : p4 == 1 ? gram[1] : p4 == 2 ? gram[2] : gram[3];
      d = __shfl(d, (q4 << 4) | i16);
      float mean = sum[0] * (1.f / D), var = fmaxf(d * (1.f / D) - mean * mean, 0.f), rstd = rsqrtf(var + EPS);
      unsigned o[4];
#pragma unroll
      for (int n = 0; n < 2; n++) {
        o[2 * n] = pk_bf16(rstd * (acc[n][0] - mean * cs[n][0]), rstd * (acc[n][1] - mean * cs[n][1]));
        o[2 * n + 1] = pk_bf16(rstd * (acc[n][2] - mean * cs[n][2]), rstd * (acc[n][3] - mean * cs[n][3]));
      }
      const int key = 64 * c + 16 * tg + i16;
      char* tile = ws + WS_KV + (size_t)(key >> 5) * KV_TILE;
      *(u32x2*)(tile + ((key & 31) + 32 * (g >> 1)) * 16 + 8 * (g & 1)) = u32x2{o[0], o[1]};
      float n2 = bf_lo(o[0]) * bf_lo(o[0]) + bf_hi(o[0]) * bf_hi(o[0]) + bf_lo(o[1]) * bf_lo(o[1]) + bf_hi(o[1]) * bf_hi(o[1]);
      if (g == 0) {
        *(unsigned*)(tile + 1024 + (key & 31) * 4) = o[2];
        *(unsigned*)(tile + 1024 + 128 + (key & 31) * 4) = o[3];
        n2 += bf_lo(o[2]) * bf_lo(o[2]) + bf_hi(o[2]) * bf_hi(o[2]) + bf_lo(o[3]) * bf_lo(o[3]) + bf_hi(o[3]) * bf_hi(o[3]);
      }
      n2 += __shfl_xor(n2, 16); n2 += __shfl_xor(n2, 32);
      n2 = fmaxf(n2, __shfl_xor(n2, 1)); n2 = fmaxf(n2, __shfl_xor(n2, 2));
      n2 = fmaxf(n2, __shfl_xor(n2, 4)); n2 = fmaxf(n2, __shfl_xor(n2, 8));
      if (lane == 0) ((float*)(ws + WS_KMAX))[4 * c + tg] = n2;
    } else {
#pragma unroll
      for (int s = 0; s < 20; s++) {
        bf16x8 xb = as_frag(xf[s]);
#pragma unroll
        for (int n = 0; n < 2; n++) {
          bf16x8 wf = frag2(tr_read(bp + s * 32 * 40 + n * 32), tr_read(bp + s * 32 * 40 + 4 * 40 + n * 32));
          acc[n] = mfma16(xb, wf, acc[n]);
          cs[n] = mfma16(ones, wf, cs[n]);
        }
        gram = mfma16(xb, xb, gram);
        sum = mfma16(xb, ones, sum);
      }
      float mean[4], rstd[4];
#pragma unroll
      for (int r = 0; r < 4; r++) {
        float d = __shfl(gram[r], (g << 4) | (4 * g + r));
        mean[r] = sum[r] * (1.f / D);
        rstd[r] = rsqrtf(fmaxf(d * (1.f / D) - mean[r] * mean[r], 0.f) + EPS);
      }
      const int key0 = 64 * c + 16 * tg;
      char* vt = ws + WS_KV + (size_t)(key0 >> 5) * KV_TILE + 1280 + (((key0 >> 4) & 1) * 2 + (g & 1)) * 336 + 8 * (g >> 1);
#pragma unroll
      for (int n = 0; n < 2; n++) {
        u32x2 o = {pk_bf16(rstd[0] * (acc[n][0] - mean[0] * cs[n][0]), rstd[1] * (acc[n][1] - mean[1] * cs[n][1])),
                   pk_bf16(rstd[2] * (acc[n][2] - mean[2] * cs[n][2]), rstd[3] * (acc[n][3] - mean[3] * cs[n][3]))};
        int d = 16 * n + i16;
        if (d == 20) o = u32x2{0x3f803f80u, 0x3f803f80u};
        if (d <= 20) *(u32x2*)(vt + d * 16) = o;
      }
    }
  }
  __syncthreads();
  if (w < 4) {
    float d = p4 == 0 ? gram[0] : p4 == 1 ? gram[1] : p4 == 2 ? gram[2] : gram[3];
    d = __shfl(d, (q4 << 4) | i16);
    float mean = sum[0] * (1.f / D), var = fmaxf(d * (1.f / D) - mean * mean, 0.f), rs = rsqrtf(var + EPS) * QSCALE;
    char* qp = ws + WS_Q + ((size_t)(64 * c + 16 * tg + i16) * D + 80 * j + 4 * g) * 2;
#pragma unroll
    for (int n = 0; n < 5; n++) {
      f32x4 cs = *(const f32x4*)(smem + K1_CS + (16 * n + 4 * g) * 4);
      u32x2 o = {pk_bf16(rs * (acc[n][0] - mean * cs[0]), rs * (acc[n][1] - mean * cs[1])),
                 pk_bf16(rs * (acc[n][2] - mean * cs[2]), rs * (acc[n][3] - mean * cs[3]))};
      *(u32x2*)(qp + 32 * n) = o;
    }
  }
}

constexpr int CH_TILES = 16, CH_BYTES = CH_TILES * KV_TILE;
constexpr int K2_SCR = 3 * CH_BYTES;
constexpr int K2_LDS = K2_SCR + 8 * 4096;

DEVI void load_chunk(const char* kv, char* buf, int ch, int w, int lane) {
  for (int p = w; p < 41; p += 8) glds16(kv + (size_t)ch * CH_BYTES + p * 1024 + lane * 16, buf + p * 1024);
}

struct KVF { u32x4 k1; unsigned k2; u32x4 v0, v1; };

__global__ void __launch_bounds__(512, 2) k2_attn(const uint16_t* __restrict__ wo, const uint16_t* __restrict__ w1,
                                                  const uint16_t* __restrict__ w2, char* __restrict__ ws) {
  extern __shared__ __attribute__((aligned(16))) char smem[];
  int c, j; map_cj(blockIdx.x, c, j);
  const int tid = threadIdx.x, lane = tid & 63, w = __builtin_amdgcn_readfirstlane(tid >> 6);
  const int r32 = lane & 31, hh = lane >> 5;
  const int head = 4 * j + (w >> 1), tok = 64 * c + 32 * (w & 1) + r32;
  const char* kv = ws + WS_KV;
  const char* qp = ws + WS_Q + ((size_t)tok * D + 20 * head) * 2;
  u32x2 qa = *(const u32x2*)(qp + 16 * hh), qb = *(const u32x2*)(qp + 16 * hh + 8);
  unsigned q2lo = *(const unsigned*)(qp + 32 + 4 * hh);
  f32x2 km = *(const f32x2*)(ws + WS_KMAX + lane * 8);
  load_chunk(kv, smem, 0, w, lane);
  {
    int task = w * 256 + blockIdx.x;
    if (task < 1160) {
      const uint16_t* src; char* dst; int ld, KS, ks, cb; bool isw1 = false;
      if (task < 200) { src = wo; dst = ws + WS_WOF; ld = D; KS = 20; ks = task / 10; cb = task - ks * 10; }
      else if (task < 680) { int t = task - 200; src = w1; dst = ws + WS_W1F; ld = F; KS = 20; ks = t / 24; cb = t - ks * 24; isw1 = true; }
      else { int t = task - 680; src = w2; dst = ws + WS_W2F; ld = D; KS = 48; ks = t / 10; cb = t - ks * 10; }
      char* scr = smem + K2_SCR + w * 4096;
      const uint16_t* sp = src + (size_t)(32 * ks + (lane >> 3)) * ld + 64 * cb + 8 * (lane & 7);
      u32x4 t0 = *(const u32x4*)(sp), t1 = *(const u32x4*)(sp + 8 * ld), t2 = *(const u32x4*)(sp + 16 * ld), t3 = *(const u32x4*)(sp + 24 * ld);
      *(u32x4*)(scr + lane * 16) = t0; *(u32x4*)(scr + 1024 + lane * 16) = t1;
      *(u32x4*)(scr + 2048 + lane * 16) = t2; *(u32x4*)(scr + 3072 + lane * 16) = t3;
      __builtin_amdgcn_fence(__ATOMIC_RELEASE, "wavefront"); __builtin_amdgcn_wave_barrier(); __builtin_amdgcn_fence(__ATOMIC_ACQUIRE, "wavefront");
      const int g = lane >> 4, q4 = (lane & 15) >> 2, p4 = lane & 3;
      const char* rp = scr + (4 * g + q4) * 128 + 8 * p4;
      u32x4 ones4 = {0x3f803f80u, 0x3f803f80u, 0x3f803f80u, 0x3f803f80u};
#pragma unroll
      for (int n = 0; n < 4; n++) {
        bf16x8 f = frag2(tr_read(rp + 32 * n), tr_read(rp + 16 * 128 + 32 * n));
        *(bf16x8*)(dst + ((size_t)((4 * cb + n) * KS + ks) * 64 + lane) * 16) = f;
        if (isw1) {
          f32x4 cs = mfma16(f, as_frag(ones4), f32x4{});
          if ((lane & 15) == 0) *(f32x4*)(ws + WS_CS1 + ((size_t)ks * F + 64 * cb + 16 * n + 4 * g) * 4) = cs;
        }
      }
    }
  }
  float kmax2 = fmaxf(km[0], km[1]);
#pragma unroll
  for (int m = 1; m < 64; m <<= 1) kmax2 = fmaxf(kmax2, __shfl_xor(kmax2, m));
  float qn = bf_lo(qa[0]) * bf_lo(qa[0]) + bf_hi(qa[0]) * bf_hi(qa[0]) + bf_lo(qa[1]) * bf_lo(qa[1]) + bf_hi(qa[1]) * bf_hi(qa[1])
           + bf_lo(qb[0]) * bf_lo(qb[0]) + bf_hi(qb[0]) * bf_hi(qb[0]) + bf_lo(qb[1]) * bf_lo(qb[1]) + bf_hi(qb[1]) * bf_hi(qb[1])
           + bf_lo(q2lo) * bf_lo(q2lo) + bf_hi(q2lo) * bf_hi(q2lo);
  qn += swap32(qn);
  const float bound = sqrtf(qn * kmax2) * 1.0078125f;
  const bf16x8 q1 = as_frag(u32x4{qa[0], qa[1], qb[0], qb[1]});
  const u32x2 q2 = {q2lo, hh == 0 ? pk_bf16(-bound, 0.f) : 0u};
  const unsigned kconst = hh == 0 ? 0x00003f80u : 0u;
  const int dsel = r32 <= 20 ? r32 : (r32 < 28 ? 20 : 16);
  const int off_k1 = lane * 16, off_k2 = 1024 + lane * 4, off_v = 1280 + hh * 336 + dsel * 16;
  __syncthreads();
  load_chunk(kv, smem + CH_BYTES, 1, w, lane);
  load_chunk(kv, smem + 2 * CH_BYTES, 2, w, lane);

  f32x16 o = {};
#pragma unroll 1
  for (int ch = 0; ch < 4; ch++) {
    const char* buf = smem + (ch == 3 ? 0 : ch) * CH_BYTES;
    if (ch == 1) { __syncthreads(); load_chunk(kv, smem, 3, w, lane); }
    if (ch == 3) __syncthreads();
    f32x16 s;
    {
      u32x4 k1 = *(const u32x4*)(buf + off_k1); unsigned k2 = *(const unsigned*)(buf + off_k2);
      s = mfma32(as_frag(k1), q1, f32x16{});
      s = mfma32k8(u32x2{k2, kconst}, q2, s);
    }
#pragma unroll 4
    for (int t = 0; t < CH_TILES; t++) {
      const char* tp = buf + t * KV_TILE;
      u32x4 v0 = *(const u32x4*)(tp + off_v), v1 = *(const u32x4*)(tp + off_v + 672);
      f32x16 sn = s;
      if (t + 1 < CH_TILES) {
        u32x4 k1 = *(const u32x4*)(tp + KV_TILE + off_k1); unsigned k2 = *(const unsigned*)(tp + KV_TILE + off_k2);
        sn = mfma32(as_frag(k1), q1, f32x16{});
        sn = mfma32k8(u32x2{k2, kconst}, q2, sn);
      }
      u32x4 p0, p1;
#pragma unroll
      for (int r = 0; r < 4; r++) p0[r] = pk_bf16(__builtin_amdgcn_exp2f(s[2 * r]), __builtin_amdgcn_exp2f(s[2 * r + 1]));
      o = mfma32(as_frag(v0), as_frag(p0), o);
#pragma unroll
      for (int r = 0; r < 4; r++) p1[r] = pk_bf16(__builtin_amdgcn_exp2f(s[8 + 2 * r]), __builtin_amdgcn_exp2f(s[8 + 2 * r + 1]));
      o = mfma32(as_frag(v1), as_frag(p1), o);
      s = sn;
    }
  }
  float l = o[8]; { float ls = swap32(l); l = hh ? l : ls; }
  if (!__all(l >= 8.6736174e-19f  )) {
    const u32x2 q2z = {q2lo, 0u};
    float m = -INFINITY;
    o = f32x16{};
#pragma unroll 1
    for (int t = 0; t < 64; t++) {
      const char* tp = kv + (size_t)t * KV_TILE;
      u32x4 k1 = *(const u32x4*)(tp + off_k1); unsigned k2 = *(const unsigned*)(tp + off_k2);
      u32x4 v0 = *(const u32x4*)(tp + off_v), v1 = *(const u32x4*)(tp + off_v + 672);
      f32x16 s = mfma32(as_frag(k1), q1, f32x16{});
      s = mfma32k8(u32x2{k2, 0u}, q2z, s);
      float tm = s[0];
#pragma unroll
      for (int r = 1; r < 16; r++) tm = fmaxf(tm, s[r]);
      tm = fmaxf(tm, swap32(tm));
      float mn = fmaxf(m, tm), alpha = __builtin_amdgcn_exp2f(m - mn);
      m = mn;
#pragma unroll
      for (int r = 0; r < 16; r++) o[r] *= alpha;
      u32x4 p0, p1;
#pragma unroll
      for (int r = 0; r < 4; r++) {
        p0[r] = pk_bf16(__builtin_amdgcn_exp2f(s[2 * r] - m), __builtin_amdgcn_exp2f(s[2 * r + 1] - m));
        p1[r] = pk_bf16(__builtin_amdgcn_exp2f(s[8 + 2 * r] - m), __builtin_amdgcn_exp2f(s[8 + 2 * r + 1] - m));
      }
      o = mfma32(as_frag(v0), as_frag(p0), o);
      o = mfma32(as_frag(v1), as_frag(p1), o);
    }
    l = o[8]; { float ls = swap32(l); l = hh ? l : ls; }
  }
  const float rl = 1.f / l;
  char* ob = ws + WS_OF + (size_t)(tok >> 4) * 20 * 1024 + (tok & 15) * 16;
#pragma unroll
  for (int q = 0; q < 3; q++) {
    if (q == 2 && hh) break;
    int col = 20 * head + 8 * q + 4 * hh;
    u32x2 v = {pk_bf16(o[4 * q] * rl, o[4 * q + 1] * rl), pk_bf16(o[4 * q + 2] * rl, o[4 * q + 3] * rl)};
    *(u32x2*)(ob + (col >> 5) * 1024 + ((col >> 2) & 3) * 256 + ((col >> 4) & 1) * 8) = v;
  }
}

template <int MODE> struct G;
template <> struct G<0> { static constexpr int KS = 20, NT = 5, NW = 4; };
template <> struct G<1> { static constexpr int KS = 20, NT = 6, NW = 8; };
template <> struct G<2> { static constexpr int KS = 48, NT = 3, NW = 8; };

template <int MODE>
__global__ void __launch_bounds__(G<MODE>::NW * 64) k_gemm(const uint16_t* __restrict__ x, const uint16_t* __restrict__ bias,
                                                           uint16_t* __restrict__ out, char* __restrict__ ws) {
  constexpr int KS = G<MODE>::KS, NT = G<MODE>::NT, NW = G<MODE>::NW;
  extern __shared__ __attribute__((aligned(16))) char smem[];
  const int tid = threadIdx.x, lane = tid & 63, w = __builtin_amdgcn_readfirstlane(tid >> 6);
  const int i16 = lane & 15, g = lane >> 4, b = blockIdx.x;
  int t16, nt0, j;
  if (MODE == 0) { int c; map_cj(b, c, j); t16 = 4 * c + w; nt0 = 5 * j; }
  else { int c = (b & 7) * 2 + (b >> 7); j = (b >> 3) & 15; t16 = 8 * c + w; nt0 = MODE == 1 ? 6 * j : (5 * j) >> 1; }
  const char* A = ws + (MODE == 0 ? WS_OF : MODE == 1 ? WS_HF : WS_H1F) + (size_t)t16 * KS * 1024 + lane * 16;
  const char* B = ws + (MODE == 0 ? WS_WOF : MODE == 1 ? WS_W1F : WS_W2F) + (size_t)nt0 * KS * 1024 + lane * 16;
  u32x4 af[KS];
#pragma unroll
  for (int s = 0; s < KS; s++) af[s] = *(const u32x4*)(A + s * 1024);
  for (int p = w; p < NT * KS; p += NW) glds16(B + p * 1024, smem + p * 1024);
  const int tok = 16 * t16 + i16;
  float mean = 0.f, rstd = 0.f;
  float* csl = (float*)(smem + NT * KS * 1024);
  if (MODE == 1) {
    f32x4 st = *(const f32x4*)(ws + WS_ST + ((size_t)tok * 16 + 4 * g) * 4);
    float s1 = st[0] + st[2], s2 = st[1] + st[3];
    s1 += __shfl_xor(s1, 16); s1 += __shfl_xor(s1, 32); s2 += __shfl_xor(s2, 16); s2 += __shfl_xor(s2, 32);
    mean = s1 * (1.f / D); rstd = rsqrtf(fmaxf(s2 * (1.f / D) - mean * mean, 0.f) + EPS);
    if (tid < 96) {
      float cs = 0.f;
#pragma unroll
      for (int s = 0; s < 20; s++) cs += ((const float*)(ws + WS_CS1))[s * F + 96 * j + tid];
      csl[tid] = cs;
    }
  }
  __syncthreads();
  f32x4 acc[NT] = {};
#pragma unroll
  for (int s = 0; s < KS; s++) {
#pragma unroll
    for (int n = 0; n < NT; n++) {
      bf16x8 wf = *(const bf16x8*)(smem + (n * KS + s) * 1024 + lane * 16);
      acc[n] = mfma16(wf, as_frag(af[s]), acc[n]);
    }
  }
  if (MODE == 0) {
    float s1 = 0.f, s2 = 0.f;
    char* hb = ws + WS_HF + (size_t)t16 * 20 * 1024 + lane * 16;
#pragma unroll
    for (int n = 0; n < NT; n++) {
      int nt = nt0 + n;
      u32x2 xv = *(const u32x2*)(x + (size_t)tok * D + 16 * nt + 4 * g);
      unsigned a0 = pk_bf16(acc[n][0], acc[n][1]), a1 = pk_bf16(acc[n][2], acc[n][3]);
      u32x2 hv = {pk_bf16(bf_lo(xv[0]) + bf_lo(a0), bf_hi(xv[0]) + bf_hi(a0)), pk_bf16(bf_lo(xv[1]) + bf_lo(a1), bf_hi(xv[1]) + bf_hi(a1))};
      *(u32x2*)(hb + (nt >> 1) * 1024 + (nt & 1) * 8) = hv;
      float h0 = bf_lo(hv[0]), h1 = bf_hi(hv[0]), h2 = bf_lo(hv[1]), h3 = bf_hi(hv[1]);
      s1 += (h0 + h1) + (h2 + h3); s2 += (h0 * h0 + h1 * h1) + (h2 * h2 + h3 * h3);
    }
    s1 += __shfl_xor(s1, 16); s1 += __shfl_xor(s1, 32); s2 += __shfl_xor(s2, 16); s2 += __shfl_xor(s2, 32);
    if (g == 0) *(f32x2*)(ws + WS_ST + ((size_t)tok * 16 + 2 * j) * 4) = f32x2{s1, s2};
  } else if (MODE == 1) {
    char* hb = ws + WS_H1F + (size_t)t16 * 48 * 1024 + lane * 16;
#pragma unroll
    for (int n2 = 0; n2 < NT / 2; n2++) {
      u32x4 ov;
#pragma unroll
      for (int e = 0; e < 2; e++) {
        int n = 2 * n2 + e;
        f32x4 cs = *(const f32x4*)(csl + 16 * n + 4 * g);
        u32x2 bv = *(const u32x2*)(bias + 16 * (nt0 + n) + 4 * g);
        float z0 = fmaxf(rstd * (acc[n][0] - mean * cs[0]) + bf_lo(bv[0]), 0.f), z1 = fmaxf(rstd * (acc[n][1] - mean * cs[1]) + bf_hi(bv[0]), 0.f);
        float z2 = fmaxf(rstd * (acc[n][2] - mean * cs[2]) + bf_lo(bv[1]), 0.f), z3 = fmaxf(rstd * (acc[n][3] - mean * cs[3]) + bf_hi(bv[1]), 0.f);
        ov[2 * e] = pk_bf16(z0 * z0, z1 * z1); ov[2 * e + 1] = pk_bf16(z2 * z2, z3 * z3);
      }
      *(u32x4*)(hb + ((nt0 >> 1) + n2) * 1024) = ov;
    }
  } else {
    const char* hb = ws + WS_HF + (size_t)t16 * 20 * 1024 + lane * 16;
#pragma unroll
    for (int n = 0; n < NT; n++) {
      int nt = nt0 + n, col = 16 * nt + 4 * g;
      if (col >= 40 * j && col < 40 * j + 40) {
        u32x2 hv = *(const u32x2*)(hb + (nt >> 1) * 1024 + (nt & 1) * 8);
        u32x2 bv = *(const u32x2*)(bias + col);
        u32x2 ov = {pk_bf16(bf_lo(hv[0]) + (acc[n][0] + bf_lo(bv[0])), bf_hi(hv[0]) + (acc[n][1] + bf_hi(bv[0]))),
                    pk_bf16(bf_lo(hv[1]) + (acc[n][2] + bf_lo(bv[1])), bf_hi(hv[1]) + (acc[n][3] + bf_hi(bv[1])))};
        *(u32x2*)(out + (size_t)tok * D + col) = ov;
      }
    }
  }
}

extern "C" void kernel_launch(void* const* d_in, const int* in_sizes, int n_in, void* d_out, int out_size, void* d_ws,
                              size_t ws_size, hipStream_t stream) {
  const uint16_t* x = (const uint16_t*)d_in[0];
  const uint16_t* wq = (const uint16_t*)d_in[1];
  const uint16_t* wk = (const uint16_t*)d_in[2];
  const uint16_t* wv = (const uint16_t*)d_in[3];
  const uint16_t* wo = (const uint16_t*)d_in[4];
  const uint16_t* w1 = (const uint16_t*)d_in[6];
  const uint16_t* b1 = (const uint16_t*)d_in[7];
  const uint16_t* w2 = (const uint16_t*)d_in[8];
  const uint16_t* b2 = (const uint16_t*)d_in[9];
  char* ws = (char*)d_ws;
  uint16_t* out = (uint16_t*)d_out;
  constexpr int L0 = 5 * 20 * 1024, L1 = 6 * 20 * 1024 + 512, L2 = 3 * 48 * 1024;
  (void)hipFuncSetAttribute((const void*)k1_qkv, hipFuncAttributeMaxDynamicSharedMemorySize, K1_LDS);
  (void)hipFuncSetAttribute((const void*)k2_attn, hipFuncAttributeMaxDynamicSharedMemorySize, K2_LDS);
  (void)hipFuncSetAttribute((const void*)k_gemm<0>, hipFuncAttributeMaxDynamicSharedMemorySize, L0);
  (void)hipFuncSetAttribute((const void*)k_gemm<1>, hipFuncAttributeMaxDynamicSharedMemorySize, L1);
  (void)hipFuncSetAttribute((const void*)k_gemm<2>, hipFuncAttributeMaxDynamicSharedMemorySize, L2);
  k1_qkv<<<256, 512, K1_LDS, stream>>>(x, wq, wk, wv, ws);
  k2_attn<<<256, 512, K2_LDS, stream>>>(wo, w1, w2, ws);
  k_gemm<0><<<256, 256, L0, stream>>>(x, nullptr, out, ws);
  k_gemm<1><<<256, 512, L1, stream>>>(x, b1, out, ws);
  k_gemm<2><<<256, 512, L2, stream>>>(x, b2, out, ws);
}
```

```cpp
#include <hip/hip_runtime.h>
#include <hip/hip_bf16.h>
#include <stdint.h>

typedef short  s16x4  __attribute__((ext_vector_type(4)));
typedef __bf16 bf16x8 __attribute__((ext_vector_type(8)));
typedef __bf16 bf16x2 __attribute__((ext_vector_type(2)));
typedef float  f32x2  __attribute__((ext_vector_type(2)));
typedef float  f32x4  __attribute__((ext_vector_type(4)));
typedef float  f32x16 __attribute__((ext_vector_type(16)));
typedef unsigned u32x2 __attribute__((ext_vector_type(2)));
typedef unsigned u32x4 __attribute__((ext_vector_type(4)));
typedef unsigned u32x8 __attribute__((ext_vector_type(8)));
typedef unsigned u32x16 __attribute__((ext_vector_type(16)));
typedef unsigned u32x32 __attribute__((ext_vector_type(32)));
typedef unsigned long long u64;
#define LAS __attribute__((address_space(3)))
#define GAS __attribute__((address_space(1)))
#define DEVI __device__ __forceinline__

constexpr int S = 2048, D = 640, F = 1536;
constexpr float QSCALE = 0.22360679774997896f * 1.4426950408889634f;
constexpr float EPS = 1e-5f;

constexpr int      KV_TILE = 2624;
constexpr unsigned WS_EPOCH = 0;
constexpr unsigned WS_FKV  = 4096;
constexpr unsigned WS_FW   = WS_FKV + 16384;
constexpr unsigned WS_HINT = WS_FW + 16384;
constexpr unsigned WS_KV   = WS_HINT + 3 * 32768;
constexpr unsigned WS_OF   = WS_KV + 176 * 1024;
constexpr unsigned WS_HF   = WS_OF + S * D * 2;
constexpr unsigned WS_H1F  = WS_HF + S * D * 2;
constexpr unsigned WS_ST   = WS_H1F + S * F * 2;
constexpr unsigned WS_WOF  = WS_OF + 256 * 48 * 1024;
constexpr unsigned WS_W1F  = WS_WOF + D * D * 2;
constexpr unsigned WS_W2F  = WS_W1F + D * F * 2;
constexpr unsigned WS_END  = WS_W2F + D * F * 2;
static_assert(WS_ST + S * 128 <= WS_WOF, "hand-off buffers");

constexpr int L_WQ = 0, L_QS = 102400, L_CS = 112640, L_KMAX = 120000, L_SY = L_CS + 400, L_XKV = 113152, L_WKV = 133632, L_PP = 99456;
constexpr int RING0 = 1152, RING_TOP = RING0 + 46 * KV_TILE;
constexpr int L_WO = 0, L_XO = 127104, L_X2 = 102400, L_H = 0, L_X3 = 81920, L_MR = 131072;
constexpr int LDS_BYTES = 163840;
constexpr unsigned SENT = 0xffffffffu;
constexpr int SPIN_MAX = 40000;

#define ST(id)
#define STC(id)

DEVI unsigned pk_bf16(float lo, float hi) {
  f32x2 v = {lo, hi};
  bf16x2 b = __builtin_convertvector(v, bf16x2);
  return __builtin_bit_cast(unsigned, b);
}
DEVI unsigned pk_out(float lo, float hi) { unsigned u = pk_bf16(lo, hi); return u < SENT ? u : SENT - 1; }
DEVI float bf_lo(unsigned u) { return __uint_as_float(u << 16); }
DEVI float bf_hi(unsigned u) { return __uint_as_float(u & 0xffff0000u); }
DEVI float sq2(unsigned u) { return bf_lo(u) * bf_lo(u) + bf_hi(u) * bf_hi(u); }
DEVI bf16x8 as_frag(u32x4 v) { return __builtin_bit_cast(bf16x8, v); }
DEVI bf16x8 frag2(s16x4 a, s16x4 b) {
  u32x2 x = __builtin_bit_cast(u32x2, a), y = __builtin_bit_cast(u32x2, b);
  u32x4 v = {x[0], x[1], y[0], y[1]};
  return __builtin_bit_cast(bf16x8, v);
}
DEVI s16x4 tr_read(const char* p) { return __builtin_amdgcn_ds_read_tr16_b64_v4i16((LAS s16x4*)p); }
DEVI f32x4 mfma16(bf16x8 a, bf16x8 b, f32x4 c) { return __builtin_amdgcn_mfma_f32_16x16x32_bf16(a, b, c, 0, 0, 0); }
DEVI f32x16 mfma32(bf16x8 a, bf16x8 b, f32x16 c) { return __builtin_amdgcn_mfma_f32_32x32x16_bf16(a, b, c, 0, 0, 0); }
DEVI f32x16 mfma32k8(u32x2 a, u32x2 b, f32x16 c) {
  return __builtin_amdgcn_mfma_f32_32x32x8bf16_1k(__builtin_bit_cast(s16x4, a), __builtin_bit_cast(s16x4, b), c, 0, 0, 0);
}
DEVI float swap32(float x, int hi) {
  auto r = __builtin_amdgcn_permlane32_swap(__float_as_uint(x), __float_as_uint(x), false, false);
  return __uint_as_float(hi ? r[0] : r[1]);
}
#define WAIT_VM0() asm volatile("s_waitcnt vmcnt(0)" ::: "memory")
#define BAR() do { asm volatile("s_waitcnt lgkmcnt(0)" ::: "memory"); __builtin_amdgcn_s_barrier(); asm volatile("" ::: "memory"); } while (0)
typedef __amdgpu_buffer_rsrc_t rsrc_t;
DEVI u32x4 ld16(rsrc_t r, unsigned voff, unsigned soff) { return __builtin_amdgcn_raw_buffer_load_b128(r, voff, soff, 16); }
DEVI u32x2 ld8(rsrc_t r, unsigned voff, unsigned soff) { return __builtin_amdgcn_raw_buffer_load_b64(r, voff, soff, 16); }
DEVI void st16(u32x4 v, rsrc_t r, unsigned voff, unsigned soff) {
  asm volatile("buffer_store_dwordx4 %0, %1, %2, %3 offen sc1\n\ts_nop 1" :: "v"(v), "v"(voff), "s"(r), "s"(soff) : "memory");
}
DEVI void st8(u32x2 v, rsrc_t r, unsigned voff, unsigned soff) { __builtin_amdgcn_raw_buffer_store_b64(v, r, voff, soff, 16); }
DEVI void st4(unsigned v, rsrc_t r, unsigned voff, unsigned soff) { __builtin_amdgcn_raw_buffer_store_b32(v, r, voff, soff, 16); }
DEVI unsigned umax3(unsigned a, unsigned b, unsigned c) { unsigned m = a > b ? a : b; return m > c ? m : c; }
DEVI unsigned umax4(unsigned m, u32x4 v) { return umax3(umax3(m, v[0], v[1]), v[2], v[3]); }
template <int CTRL> DEVI float dpp(float x) { return __builtin_bit_cast(float, __builtin_amdgcn_mov_dpp(__builtin_bit_cast(int, x), CTRL, 0xf, 0xf, true)); }
DEVI float row16_sum(float x) { x += dpp<0xB1>(x); x += dpp<0x4E>(x); x += dpp<0x141>(x); x += dpp<0x128>(x); return x; }
DEVI float row16_max(float x) { x = fmaxf(x, dpp<0xB1>(x)); x = fmaxf(x, dpp<0x4E>(x)); x = fmaxf(x, dpp<0x141>(x)); x = fmaxf(x, dpp<0x128>(x)); return x; }
DEVI float xrow_sum(float x) {
  auto s = __builtin_amdgcn_permlane16_swap(__float_as_uint(x), __float_as_uint(x), false, false);
  x = __uint_as_float(s[0]) + __uint_as_float(s[1]);
  auto t = __builtin_amdgcn_permlane32_swap(__float_as_uint(x), __float_as_uint(x), false, false);
  return __uint_as_float(t[0]) + __uint_as_float(t[1]);
}
DEVI float xrow_max(float x) {
  auto s = __builtin_amdgcn_permlane16_swap(__float_as_uint(x), __float_as_uint(x), false, false);
  x = fmaxf(__uint_as_float(s[0]), __uint_as_float(s[1]));
  auto t = __builtin_amdgcn_permlane32_swap(__float_as_uint(x), __float_as_uint(x), false, false);
  return fmaxf(__uint_as_float(t[0]), __uint_as_float(t[1]));
}
DEVI void pause(unsigned ticks) {
  const u64 t0 = __builtin_amdgcn_s_memrealtime();
  while (__builtin_amdgcn_s_memrealtime() - t0 < ticks) __builtin_amdgcn_s_sleep(1);
}
#define PIN(x) asm volatile("" : "+v"(x))
DEVI rsrc_t mk_rsrc(const void* p, int bytes) { return __builtin_amdgcn_make_buffer_rsrc((void*)p, 0, bytes, 0x00020000); }
DEVI u32x4 in16(rsrc_t r, unsigned voff, unsigned soff) { return __builtin_amdgcn_raw_buffer_load_b128(r, voff, soff, 0); }
DEVI u32x2 in8(rsrc_t r, unsigned voff, unsigned soff) { return __builtin_amdgcn_raw_buffer_load_b64(r, voff, soff, 0); }
#define DMA1(SC, r, voff, soff, lds, IMM) asm volatile("s_mov_b32 m0, %3\n\ts_nop 0\n\tbuffer_load_dwordx4 %0, %1, %2 offen offset:%4" SC " lds" :: "v"(voff), "s"(r), "s"(soff), "s"(lds), "i"(IMM) : "memory", "m0")
#define DMA4(SC, r, voff, soff, lds) asm volatile("s_mov_b32 m0, %3\n\ts_nop 0\n\tbuffer_load_dwordx4 %0, %1, %2 offen" SC " lds\n\tbuffer_load_dwordx4 %0, %1, %2 offen offset:1024" SC " lds\n\t" \
    "buffer_load_dwordx4 %0, %1, %2 offen offset:2048" SC " lds\n\tbuffer_load_dwordx4 %0, %1, %2 offen offset:3072" SC " lds" :: "v"(voff), "s"(r), "s"(soff), "s"(lds) : "memory", "m0")
#define DMA2(SC, r, voff, soff, lds) asm volatile("s_mov_b32 m0, %3\n\ts_nop 0\n\tbuffer_load_dwordx4 %0, %1, %2 offen" SC " lds\n\tbuffer_load_dwordx4 %0, %1, %2 offen offset:1024" SC " lds" \
    :: "v"(voff), "s"(r), "s"(soff), "s"(lds) : "memory", "m0")
#pragma clang diagnostic ignored "-Winline-asm"
#define ATT_ALL \
  "ds_read_b128 v[232:235], v252\n\t" \
  "ds_read_b32 v236, v253\n\t" \
  "ds_read_b128 v[240:243], v254\n\t" \
  "v_mov_b32 v228, 0\n\t" \
  "v_mov_b32 v229, 0\n\t" \
  "v_mov_b32 v230, 0\n\t" \
  "v_mov_b32 v231, 0\n\t" \
  "v_mov_b32 v244, 0\n\t" \
  "v_mov_b32 v245, 0\n\t" \
  "v_mov_b32 v246, 0\n\t" \
  "v_mov_b32 v247, 0\n\t" \
  "v_mov_b32 v32, -1\n\t" \
  "v_mov_b32 v33, -1\n\t" \
  "v_mov_b32 v34, -1\n\t" \
  "v_mov_b32 v35, -1\n\t" \
  "s_waitcnt lgkmcnt(1)\n\t" \
  "v_mfma_f32_32x32x16_bf16 v[176:191], v[232:235], v[248:251], 0\n\t" \
  "v_mfma_f32_32x32x8_bf16 v[176:191], v[236:237], v[238:239], v[176:191]\n\t" \
  "v_mov_b32 v208, 0\n\t" \
  "v_mov_b32 v209, 0\n\t" \
  "v_mov_b32 v210, 0\n\t" \
  "v_mov_b32 v211, 0\n\t" \
  "v_mov_b32 v212, 0\n\t" \
  "v_mov_b32 v213, 0\n\t" \
  "v_mov_b32 v214, 0\n\t" \
  "v_mov_b32 v215, 0\n\t" \
  "v_mov_b32 v216, 0\n\t" \
  "v_mov_b32 v217, 0\n\t" \
  "v_mov_b32 v218, 0\n\t" \
  "v_mov_b32 v219, 0\n\t" \
  "v_mov_b32 v220, 0\n\t" \
  "v_mov_b32 v221, 0\n\t" \
  "v_mov_b32 v222, 0\n\t" \
  "v_mov_b32 v223, 0\n\t" \
  "v_mov_b32 v152, 0\n\t" \
  "v_mov_b32 v153, 0\n\t" \
  "v_mov_b32 v154, 0\n\t" \
  "v_mov_b32 v155, 0\n\t" \
  "v_mov_b32 v156, 0\n\t" \
  "v_mov_b32 v157, 0\n\t" \
  "v_mov_b32 v158, 0\n\t" \
  "v_mov_b32 v159, 0\n\t" \
  "v_mov_b32 v160, 0\n\t" \
  "v_mov_b32 v161, 0\n\t" \
  "v_mov_b32 v162, 0\n\t" \
  "v_mov_b32 v163, 0\n\t" \
  "v_mov_b32 v164, 0\n\t" \
  "v_mov_b32 v165, 0\n\t" \
  "v_mov_b32 v166, 0\n\t" \
  "v_mov_b32 v167, 0\n\t" \
  "s_cmp_eq_u32 %[ppk], 0\n\t" \
  "s_cbranch_scc1 att_pp0_%=\n\t" \
  "s_add_u32 m0, %[ppm], 0\n\t" \
  "s_nop 0\n\t" \
  "buffer_load_dwordx4 v174, %[ppsrd], %[pps0] offen lds\n\t" \
  "s_add_u32 m0, %[ppm], 1024\n\t" \
  "s_nop 0\n\t" \
  "buffer_load_dwordx4 v174, %[ppsrd], %[pps1] offen lds\n\t" \
  "s_add_u32 m0, %[ppm], 2048\n\t" \
  "s_nop 0\n\t" \
  "buffer_load_dwordx4 v174, %[ppsrd], %[pps2] offen lds\n\t" \
  "s_add_u32 m0, %[ppm], 3072\n\t" \
  "s_nop 0\n\t" \
  "buffer_load_dwordx4 v174, %[ppsrd], %[pps3] offen lds\n\t" \
  "att_pp0_%=:\n\t" \
  "v_exp_f32 v176, v176\n\t" \
  "v_exp_f32 v177, v177\n\t" \
  "v_mfma_f32_32x32x16_bf16 v[152:167], v[244:247], v[228:231], v[152:167]\n\t" \
  "ds_read_b128 v[244:247], v254 offset:672\n\t" \
  "v_exp_f32 v178, v178\n\t" \
  "v_exp_f32 v179, v179\n\t" \
  "v_exp_f32 v180, v180\n\t" \
  "v_exp_f32 v181, v181\n\t" \
  "v_mfma_f32_32x32x16_bf16 v[192:207], v[232:235], v[168:171], 0\n\t" \
  "ds_read_b128 v[232:235], v252 offset:5248\n\t" \
  "v_cvt_pk_bf16_f32 v224, v176, v177\n\t" \
  "v_cvt_pk_bf16_f32 v225, v178, v179\n\t" \
  "v_exp_f32 v182, v182\n\t" \
  "s_mov_b32 m0, %[kvm]\n\t" \
  "s_mov_b32 %[t0], %[kvs]\n\t" \
  "buffer_load_dwordx4 v255, %[srd], %[t0] offen offset:0 sc1 lds\n\t" \
  "v_exp_f32 v183, v183\n\t" \
  "v_exp_f32 v184, v184\n\t" \
  "v_exp_f32 v185, v185\n\t" \
  "v_mfma_f32_32x32x8_bf16 v[192:207], v[236:237], v[172:173], v[192:207]\n\t" \
  "ds_read_b32 v236, v253 offset:5248\n\t" \
  "v_cvt_pk_bf16_f32 v226, v180, v181\n\t" \
  "v_cvt_pk_bf16_f32 v227, v182, v183\n\t" \
  "v_exp_f32 v186, v186\n\t" \
  "v_exp_f32 v187, v187\n\t" \
  "v_exp_f32 v188, v188\n\t" \
  "v_exp_f32 v189, v189\n\t" \
  "s_waitcnt lgkmcnt(3)\n\t" \
  "v_mfma_f32_32x32x16_bf16 v[208:223], v[240:243], v[224:227], v[208:223]\n\t" \
  "v_cvt_pk_bf16_f32 v228, v184, v185\n\t" \
  "v_cvt_pk_bf16_f32 v229, v186, v187\n\t" \
  "v_exp_f32 v190, v190\n\t" \
  "buffer_load_dwordx4 v255, %[srd], %[t0] offen offset:1024 sc1 lds\n\t" \
  "v_exp_f32 v191, v191\n\t" \
  "v_cvt_pk_bf16_f32 v230, v188, v189\n\t" \
  "v_cvt_pk_bf16_f32 v231, v190, v191\n\t" \
  "v_exp_f32 v192, v192\n\t" \
  "v_exp_f32 v193, v193\n\t" \
  "s_waitcnt lgkmcnt(0)\n\t" \
  "v_mfma_f32_32x32x16_bf16 v[208:223], v[244:247], v[228:231], v[208:223]\n\t" \
  "v_exp_f32 v194, v194\n\t" \
  "v_exp_f32 v195, v195\n\t" \
  "v_exp_f32 v196, v196\n\t" \
  "v_exp_f32 v197, v197\n\t" \
  "v_mfma_f32_32x32x16_bf16 v[176:191], v[232:235], v[248:251], 0\n\t" \
  "v_cvt_pk_bf16_f32 v224, v192, v193\n\t" \
  "v_cvt_pk_bf16_f32 v225, v194, v195\n\t" \
  "v_exp_f32 v198, v198\n\t" \
  "buffer_load_dwordx4 v255, %[srd], %[t0] offen offset:2048 sc1 lds\n\t" \
  "v_exp_f32 v199, v199\n\t" \
  "v_exp_f32 v200, v200\n\t" \
  "v_exp_f32 v201, v201\n\t" \
  "v_mfma_f32_32x32x8_bf16 v[176:191], v[236:237], v[238:239], v[176:191]\n\t" \
  "v_cvt_pk_bf16_f32 v226, v196, v197\n\t" \
  "v_cvt_pk_bf16_f32 v227, v198, v199\n\t" \
  "v_exp_f32 v202, v202\n\t" \
  "v_exp_f32 v203, v203\n\t" \
  "v_exp_f32 v204, v204\n\t" \
  "v_exp_f32 v205, v205\n\t" \
  "v_mfma_f32_32x32x16_bf16 v[152:167], v[240:243], v[224:227], v[152:167]\n\t" \
  "ds_read_b128 v[240:243], v254 offset:5248\n\t" \
  "v_cvt_pk_bf16_f32 v228, v200, v201\n\t" \
  "v_cvt_pk_bf16_f32 v229, v202, v203\n\t" \
  "v_exp_f32 v206, v206\n\t" \
  "buffer_load_dwordx4 v255, %[srd], %[t0] offen offset:3072 sc1 lds\n\t" \
  "v_exp_f32 v207, v207\n\t" \
  "v_cvt_pk_bf16_f32 v230, v204, v205\n\t" \
  "v_cvt_pk_bf16_f32 v231, v206, v207\n\t" \
  "v_exp_f32 v176, v176\n\t" \
  "v_exp_f32 v177, v177\n\t" \
  "v_mfma_f32_32x32x16_bf16 v[152:167], v[244:247], v[228:231], v[152:167]\n\t" \
  "ds_read_b128 v[244:247], v254 offset:5920\n\t" \
  "v_exp_f32 v178, v178\n\t" \
  "v_exp_f32 v179, v179\n\t" \
  "v_exp_f32 v180, v180\n\t" \
  "v_exp_f32 v181, v181\n\t" \
  "v_mfma_f32_32x32x16_bf16 v[192:207], v[232:235], v[168:171], 0\n\t" \
  "ds_read_b128 v[232:235], v252 offset:10496\n\t" \
  "v_cvt_pk_bf16_f32 v224, v176, v177\n\t" \
  "v_cvt_pk_bf16_f32 v225, v178, v179\n\t" \
  "v_exp_f32 v182, v182\n\t" \
  "s_add_u32 m0, m0, 0x1000\n\t" \
  "s_add_u32 %[t0], %[t0], 0x1000\n\t" \
  "buffer_load_dwordx4 v255, %[srd], %[t0] offen offset:0 sc1 lds\n\t" \
  "v_exp_f32 v183, v183\n\t" \
  "v_exp_f32 v184, v184\n\t" \
  "v_exp_f32 v185, v185\n\t" \
  "v_mfma_f32_32x32x8_bf16 v[192:207], v[236:237], v[172:173], v[192:207]\n\t" \
  "ds_read_b32 v236, v253 offset:10496\n\t" \
  "v_cvt_pk_bf16_f32 v226, v180, v181\n\t" \
  "v_cvt_pk_bf16_f32 v227, v182, v183\n\t" \
  "v_exp_f32 v186, v186\n\t" \
  "v_exp_f32 v187, v187\n\t" \
  "v_exp_f32 v188, v188\n\t" \
  "v_exp_f32 v189, v189\n\t" \
  "s_waitcnt lgkmcnt(3)\n\t" \
  "v_mfma_f32_32x32x16_bf16 v[208:223], v[240:243], v[224:227], v[208:223]\n\t" \
  "v_cvt_pk_bf16_f32 v228, v184, v185\n\t" \
  "v_cvt_pk_bf16_f32 v229, v186, v187\n\t" \
  "v_exp_f32 v190, v190\n\t" \
  "buffer_load_dwordx4 v255, %[srd], %[t0] offen offset:1024 sc1 lds\n\t" \
  "v_exp_f32 v191, v191\n\t" \
  "v_cvt_pk_bf16_f32 v230, v188, v189\n\t" \
  "v_cvt_pk_bf16_f32 v231, v190, v191\n\t" \
  "v_exp_f32 v192, v192\n\t" \
  "v_exp_f32 v193, v193\n\t" \
  "s_waitcnt lgkmcnt(0)\n\t" \
  "v_mfma_f32_32x32x16_bf16 v[208:223], v[244:247], v[228:231], v[208:223]\n\t" \
  "v_exp_f32 v194, v194\n\t" \
  "v_exp_f32 v195, v195\n\t" \
  "v_exp_f32 v196, v196\n\t" \
  "v_exp_f32 v197, v197\n\t" \
  "v_mfma_f32_32x32x16_bf16 v[176:191], v[232:235], v[248:251], 0\n\t" \
  "v_cvt_pk_bf16_f32 v224, v192, v193\n\t" \
  "v_cvt_pk_bf16_f32 v225, v194, v195\n\t" \
  "v_exp_f32 v198, v198\n\t" \
  "buffer_load_dwordx4 v255, %[srd], %[t0] offen offset:2048 sc1 lds\n\t" \
  "v_exp_f32 v199, v199\n\t" \
  "v_exp_f32 v200, v200\n\t" \
  "v_exp_f32 v201, v201\n\t" \
  "v_mfma_f32_32x32x8_bf16 v[176:191], v[236:237], v[238:239], v[176:191]\n\t" \
  "v_cvt_pk_bf16_f32 v226, v196, v197\n\t" \
  "v_cvt_pk_bf16_f32 v227, v198, v199\n\t" \
  "v_exp_f32 v202, v202\n\t" \
  "v_exp_f32 v203, v203\n\t" \
  "v_exp_f32 v204, v204\n\t" \
  "v_exp_f32 v205, v205\n\t" \
  "v_mfma_f32_32x32x16_bf16 v[152:167], v[240:243], v[224:227], v[152:167]\n\t" \
  "ds_read_b128 v[240:243], v254 offset:10496\n\t" \
  "v_cvt_pk_bf16_f32 v228, v200, v201\n\t" \
  "v_cvt_pk_bf16_f32 v229, v202, v203\n\t" \
  "v_exp_f32 v206, v206\n\t" \
  "buffer_load_dwordx4 v255, %[srd], %[t0] offen offset:3072 sc1 lds\n\t" \
  "v_exp_f32 v207, v207\n\t" \
  "v_cvt_pk_bf16_f32 v230, v204, v205\n\t" \
  "v_cvt_pk_bf16_f32 v231, v206, v207\n\t" \
  "v_exp_f32 v176, v176\n\t" \
  "v_exp_f32 v177, v177\n\t" \
  "v_mfma_f32_32x32x16_bf16 v[152:167], v[244:247], v[228:231], v[152:167]\n\t" \
  "ds_read_b128 v[244:247], v254 offset:11168\n\t" \
  "v_exp_f32 v178, v178\n\t" \
  "v_exp_f32 v179, v179\n\t" \
  "v_exp_f32 v180, v180\n\t" \
  "v_exp_f32 v181, v181\n\t" \
  "v_mfma_f32_32x32x16_bf16 v[192:207], v[232:235], v[168:171], 0\n\t" \
  "ds_read_b128 v[232:235], v252 offset:15744\n\t" \
  "v_cvt_pk_bf16_f32 v224, v176, v177\n\t" \
  "v_cvt_pk_bf16_f32 v225, v178, v179\n\t" \
  "v_exp_f32 v182, v182\n\t" \
  "s_add_u32 m0, m0, 0x1000\n\t" \
  "s_add_u32 %[t0], %[t0], 0x1000\n\t" \
  "buffer_load_dwordx4 v255, %[srd], %[t0] offen offset:0 sc1 lds\n\t" \
  "v_exp_f32 v183, v183\n\t" \
  "v_exp_f32 v184, v184\n\t" \
  "v_exp_f32 v185, v185\n\t" \
  "v_mfma_f32_32x32x8_bf16 v[192:207], v[236:237], v[172:173], v[192:207]\n\t" \
  "ds_read_b32 v236, v253 offset:15744\n\t" \
  "v_cvt_pk_bf16_f32 v226, v180, v181\n\t" \
  "v_cvt_pk_bf16_f32 v227, v182, v183\n\t" \
  "v_exp_f32 v186, v186\n\t" \
  "v_exp_f32 v187, v187\n\t" \
  "v_exp_f32 v188, v188\n\t" \
  "v_exp_f32 v189, v189\n\t" \
  "s_waitcnt lgkmcnt(3)\n\t" \
  "v_mfma_f32_32x32x16_bf16 v[208:223], v[240:243], v[224:227], v[208:223]\n\t" \
  "v_cvt_pk_bf16_f32 v228, v184, v185\n\t" \
  "v_cvt_pk_bf16_f32 v229, v186, v187\n\t" \
  "v_exp_f32 v190, v190\n\t" \
  "buffer_load_dwordx4 v255, %[srd], %[t0] offen offset:1024 sc1 lds\n\t" \
  "v_exp_f32 v191, v191\n\t" \
  "v_cvt_pk_bf16_f32 v230, v188, v189\n\t" \
  "v_cvt_pk_bf16_f32 v231, v190, v191\n\t" \
  "v_exp_f32 v192, v192\n\t" \
  "v_exp_f32 v193, v193\n\t" \
  "s_waitcnt lgkmcnt(0)\n\t" \
  "v_mfma_f32_32x32x16_bf16 v[208:223], v[244:247], v[228:231], v[208:223]\n\t" \
  "v_exp_f32 v194, v194\n\t" \
  "v_exp_f32 v195, v195\n\t" \
  "v_exp_f32 v196, v196\n\t" \
  "v_exp_f32 v197, v197\n\t" \
  "v_mfma_f32_32x32x16_bf16 v[176:191], v[232:235], v[248:251], 0\n\t" \
  "v_cvt_pk_bf16_f32 v224, v192, v193\n\t" \
  "v_cvt_pk_bf16_f32 v225, v194, v195\n\t" \
  "v_exp_f32 v198, v198\n\t" \
  "buffer_load_dwordx4 v255, %[srd], %[t0] offen offset:2048 sc1 lds\n\t" \
  "v_exp_f32 v199, v199\n\t" \
  "v_exp_f32 v200, v200\n\t" \
  "v_exp_f32 v201, v201\n\t" \
  "v_mfma_f32_32x32x8_bf16 v[176:191], v[236:237], v[238:239], v[176:191]\n\t" \
  "v_cvt_pk_bf16_f32 v226, v196, v197\n\t" \
  "v_cvt_pk_bf16_f32 v227, v198, v199\n\t" \
  "v_exp_f32 v202, v202\n\t" \
  "v_exp_f32 v203, v203\n\t" \
  "v_exp_f32 v204, v204\n\t" \
  "v_exp_f32 v205, v205\n\t" \
  "v_mfma_f32_32x32x16_bf16 v[152:167], v[240:243], v[224:227], v[152:167]\n\t" \
  "ds_read_b128 v[240:243], v254 offset:15744\n\t" \
  "v_cvt_pk_bf16_f32 v228, v200, v201\n\t" \
  "v_cvt_pk_bf16_f32 v229, v202, v203\n\t" \
  "v_exp_f32 v206, v206\n\t" \
  "buffer_load_dwordx4 v255, %[srd], %[t0] offen offset:3072 sc1 lds\n\t" \
  "v_exp_f32 v207, v207\n\t" \
  "v_cvt_pk_bf16_f32 v230, v204, v205\n\t" \
  "v_cvt_pk_bf16_f32 v231, v206, v207\n\t" \
  "v_exp_f32 v176, v176\n\t" \
  "v_exp_f32 v177, v177\n\t" \
  "v_mfma_f32_32x32x16_bf16 v[152:167], v[244:247], v[228:231], v[152:167]\n\t" \
  "ds_read_b128 v[244:247], v254 offset:16416\n\t" \
  "v_exp_f32 v178, v178\n\t" \
  "v_exp_f32 v179, v179\n\t" \
  "v_exp_f32 v180, v180\n\t" \
  "v_exp_f32 v181, v181\n\t" \
  "v_mfma_f32_32x32x16_bf16 v[192:207], v[232:235], v[168:171], 0\n\t" \
  "v_add_u32 v252, 0x5200, v252\n\t" \
  "ds_read_b128 v[232:235], v252 offset:0\n\t" \
  "v_cvt_pk_bf16_f32 v224, v176, v177\n\t" \
  "v_cvt_pk_bf16_f32 v225, v178, v179\n\t" \
  "v_exp_f32 v182, v182\n\t" \
  "buffer_store_dwordx4 v[32:35], v255, %[srd], %[clr] offen offset:0 sc1\n\t" \
  "v_exp_f32 v183, v183\n\t" \
  "v_exp_f32 v184, v184\n\t" \
  "v_exp_f32 v185, v185\n\t" \
  "v_mfma_f32_32x32x8_bf16 v[192:207], v[236:237], v[172:173], v[192:207]\n\t" \
  "v_add_u32 v253, 0x5200, v253\n\t" \
  "ds_read_b32 v236, v253 offset:0\n\t" \
  "v_cvt_pk_bf16_f32 v226, v180, v181\n\t" \
  "v_cvt_pk_bf16_f32 v227, v182, v183\n\t" \
  "v_exp_f32 v186, v186\n\t" \
  "v_exp_f32 v187, v187\n\t" \
  "v_exp_f32 v188, v188\n\t" \
  "v_exp_f32 v189, v189\n\t" \
  "s_waitcnt lgkmcnt(3)\n\t" \
  "v_mfma_f32_32x32x16_bf16 v[208:223], v[240:243], v[224:227], v[208:223]\n\t" \
  "v_cvt_pk_bf16_f32 v228, v184, v185\n\t" \
  "v_cvt_pk_bf16_f32 v229, v186, v187\n\t" \
  "v_exp_f32 v190, v190\n\t" \
  "v_exp_f32 v191, v191\n\t" \
  "v_cvt_pk_bf16_f32 v230, v188, v189\n\t" \
  "v_cvt_pk_bf16_f32 v231, v190, v191\n\t" \
  "v_exp_f32 v192, v192\n\t" \
  "v_exp_f32 v193, v193\n\t" \
  "s_waitcnt lgkmcnt(0)\n\t" \
  "v_mfma_f32_32x32x16_bf16 v[208:223], v[244:247], v[228:231], v[208:223]\n\t" \
  "v_exp_f32 v194, v194\n\t" \
  "v_exp_f32 v195, v195\n\t" \
  "v_exp_f32 v196, v196\n\t" \
  "v_exp_f32 v197, v197\n\t" \
  "v_mfma_f32_32x32x16_bf16 v[176:191], v[232:235], v[248:251], 0\n\t" \
  "v_cvt_pk_bf16_f32 v224, v192, v193\n\t" \
  "v_cvt_pk_bf16_f32 v225, v194, v195\n\t" \
  "v_exp_f32 v198, v198\n\t" \
  "buffer_store_dwordx4 v[32:35], v255, %[srd], %[clr] offen offset:1024 sc1\n\t" \
  "v_exp_f32 v199, v199\n\t" \
  "v_exp_f32 v200, v200\n\t" \
  "v_exp_f32 v201, v201\n\t" \
  "v_mfma_f32_32x32x8_bf16 v[176:191], v[236:237], v[238:239], v[176:191]\n\t" \
  "v_cvt_pk_bf16_f32 v226, v196, v197\n\t" \
  "v_cvt_pk_bf16_f32 v227, v198, v199\n\t" \
  "v_exp_f32 v202, v202\n\t" \
  "v_exp_f32 v203, v203\n\t" \
  "v_exp_f32 v204, v204\n\t" \
  "v_exp_f32 v205, v205\n\t" \
  "v_mfma_f32_32x32x16_bf16 v[152:167], v[240:243], v[224:227], v[152:167]\n\t" \
  "v_add_u32 v254, 0x5200, v254\n\t" \
  "ds_read_b128 v[240:243], v254 offset:0\n\t" \
  "v_cvt_pk_bf16_f32 v228, v200, v201\n\t" \
  "v_cvt_pk_bf16_f32 v229, v202, v203\n\t" \
  "v_exp_f32 v206, v206\n\t" \
  "v_exp_f32 v207, v207\n\t" \
  "v_cvt_pk_bf16_f32 v230, v204, v205\n\t" \
  "v_cvt_pk_bf16_f32 v231, v206, v207\n\t" \
  "v_exp_f32 v176, v176\n\t" \
  "v_exp_f32 v177, v177\n\t" \
  "v_mfma_f32_32x32x16_bf16 v[152:167], v[244:247], v[228:231], v[152:167]\n\t" \
  "ds_read_b128 v[244:247], v254 offset:672\n\t" \
  "v_exp_f32 v178, v178\n\t" \
  "v_exp_f32 v179, v179\n\t" \
  "v_exp_f32 v180, v180\n\t" \
  "v_exp_f32 v181, v181\n\t" \
  "v_mfma_f32_32x32x16_bf16 v[192:207], v[232:235], v[168:171], 0\n\t" \
  "ds_read_b128 v[232:235], v252 offset:5248\n\t" \
  "v_cvt_pk_bf16_f32 v224, v176, v177\n\t" \
  "v_cvt_pk_bf16_f32 v225, v178, v179\n\t" \
  "v_exp_f32 v182, v182\n\t" \
  "buffer_store_dwordx4 v[32:35], v255, %[srd], %[clr] offen offset:2048 sc1\n\t" \
  "v_exp_f32 v183, v183\n\t" \
  "v_exp_f32 v184, v184\n\t" \
  "v_exp_f32 v185, v185\n\t" \
  "v_mfma_f32_32x32x8_bf16 v[192:207], v[236:237], v[172:173], v[192:207]\n\t" \
  "ds_read_b32 v236, v253 offset:5248\n\t" \
  "v_cvt_pk_bf16_f32 v226, v180, v181\n\t" \
  "v_cvt_pk_bf16_f32 v227, v182, v183\n\t" \
  "v_exp_f32 v186, v186\n\t" \
  "v_exp_f32 v187, v187\n\t" \
  "v_exp_f32 v188, v188\n\t" \
  "v_exp_f32 v189, v189\n\t" \
  "s_waitcnt lgkmcnt(3)\n\t" \
  "v_mfma_f32_32x32x16_bf16 v[208:223], v[240:243], v[224:227], v[208:223]\n\t" \
  "v_cvt_pk_bf16_f32 v228, v184, v185\n\t" \
  "v_cvt_pk_bf16_f32 v229, v186, v187\n\t" \
  "v_exp_f32 v190, v190\n\t" \
  "v_exp_f32 v191, v191\n\t" \
  "v_cvt_pk_bf16_f32 v230, v188, v189\n\t" \
  "v_cvt_pk_bf16_f32 v231, v190, v191\n\t" \
  "v_exp_f32 v192, v192\n\t" \
  "v_exp_f32 v193, v193\n\t" \
  "s_waitcnt lgkmcnt(0)\n\t" \
  "v_mfma_f32_32x32x16_bf16 v[208:223], v[244:247], v[228:231], v[208:223]\n\t" \
  "v_exp_f32 v194, v194\n\t" \
  "v_exp_f32 v195, v195\n\t" \
  "v_exp_f32 v196, v196\n\t" \
  "v_exp_f32 v197, v197\n\t" \
  "v_mfma_f32_32x32x16_bf16 v[176:191], v[232:235], v[248:251], 0\n\t" \
  "v_cvt_pk_bf16_f32 v224, v192, v193\n\t" \
  "v_cvt_pk_bf16_f32 v225, v194, v195\n\t" \
  "v_exp_f32 v198, v198\n\t" \
  "buffer_store_dwordx4 v[32:35], v255, %[srd], %[clr] offen offset:3072 sc1\n\t" \
  "v_exp_f32 v199, v199\n\t" \
  "v_exp_f32 v200, v200\n\t" \
  "v_exp_f32 v201, v201\n\t" \
  "v_mfma_f32_32x32x8_bf16 v[176:191], v[236:237], v[238:239], v[176:191]\n\t" \
  "v_cvt_pk_bf16_f32 v226, v196, v197\n\t" \
  "v_cvt_pk_bf16_f32 v227, v198, v199\n\t" \
  "v_exp_f32 v202, v202\n\t" \
  "v_exp_f32 v203, v203\n\t" \
  "v_exp_f32 v204, v204\n\t" \
  "v_exp_f32 v205, v205\n\t" \
  "v_mfma_f32_32x32x16_bf16 v[152:167], v[240:243], v[224:227], v[152:167]\n\t" \
  "ds_read_b128 v[240:243], v254 offset:5248\n\t" \
  "v_cvt_pk_bf16_f32 v228, v200, v201\n\t" \
  "v_cvt_pk_bf16_f32 v229, v202, v203\n\t" \
  "v_exp_f32 v206, v206\n\t" \
  "v_exp_f32 v207, v207\n\t" \
  "v_cvt_pk_bf16_f32 v230, v204, v205\n\t" \
  "v_cvt_pk_bf16_f32 v231, v206, v207\n\t" \
  "v_exp_f32 v176, v176\n\t" \
  "v_exp_f32 v177, v177\n\t" \
  "v_mfma_f32_32x32x16_bf16 v[152:167], v[244:247], v[228:231], v[152:167]\n\t" \
  "ds_read_b128 v[244:247], v254 offset:5920\n\t" \
  "v_exp_f32 v178, v178\n\t" \
  "v_exp_f32 v179, v179\n\t" \
  "v_exp_f32 v180, v180\n\t" \
  "v_exp_f32 v181, v181\n\t" \
  "v_mfma_f32_32x32x16_bf16 v[192:207], v[232:235], v[168:171], 0\n\t" \
  "ds_read_b128 v[232:235], v252 offset:10496\n\t" \
  "v_cvt_pk_bf16_f32 v224, v176, v177\n\t" \
  "v_cvt_pk_bf16_f32 v225, v178, v179\n\t" \
  "v_exp_f32 v182, v182\n\t" \
  "s_add_u32 %[clr], %[clr], 0x1000\n\t" \
  "buffer_store_dwordx4 v[32:35], v255, %[srd], %[clr] offen offset:0 sc1\n\t" \
  "v_exp_f32 v183, v183\n\t" \
  "v_exp_f32 v184, v184\n\t" \
  "v_exp_f32 v185, v185\n\t" \
  "v_mfma_f32_32x32x8_bf16 v[192:207], v[236:237], v[172:173], v[192:207]\n\t" \
  "ds_read_b32 v236, v253 offset:10496\n\t" \
  "v_cvt_pk_bf16_f32 v226, v180, v181\n\t" \
  "v_cvt_pk_bf16_f32 v227, v182, v183\n\t" \
  "v_exp_f32 v186, v186\n\t" \
  "v_exp_f32 v187, v187\n\t" \
  "v_exp_f32 v188, v188\n\t" \
  "v_exp_f32 v189, v189\n\t" \
  "s_waitcnt lgkmcnt(3)\n\t" \
  "v_mfma_f32_32x32x16_bf16 v[208:223], v[240:243], v[224:227], v[208:223]\n\t" \
  "v_cvt_pk_bf16_f32 v228, v184, v185\n\t" \
  "v_cvt_pk_bf16_f32 v229, v186, v187\n\t" \
  "v_exp_f32 v190, v190\n\t" \
  "v_exp_f32 v191, v191\n\t" \
  "v_cvt_pk_bf16_f32 v230, v188, v189\n\t" \
  "v_cvt_pk_bf16_f32 v231, v190, v191\n\t" \
  "v_exp_f32 v192, v192\n\t" \
  "v_exp_f32 v193, v193\n\t" \
  "s_waitcnt lgkmcnt(0)\n\t" \
  "v_mfma_f32_32x32x16_bf16 v[208:223], v[244:247], v[228:231], v[208:223]\n\t" \
  "v_exp_f32 v194, v194\n\t" \
  "v_exp_f32 v195, v195\n\t" \
  "v_exp_f32 v196, v196\n\t" \
  "v_exp_f32 v197, v197\n\t" \
  "v_mfma_f32_32x32x16_bf16 v[176:191], v[232:235], v[248:251], 0\n\t" \
  "v_cvt_pk_bf16_f32 v224, v192, v193\n\t" \
  "v_cvt_pk_bf16_f32 v225, v194, v195\n\t" \
  "v_exp_f32 v198, v198\n\t" \
  "buffer_store_dwordx4 v[32:35], v255, %[srd], %[clr] offen offset:1024 sc1\n\t" \
  "s_cmp_eq_u32 %[ppk], 0\n\t" \
  "s_cbranch_scc1 att_pp1_%=\n\t" \
  "s_waitcnt vmcnt(18)\n\t" \
  "ds_read_b64_tr_b16 v[48:49], v175 offset:0\n\t" \
  "ds_read_b64_tr_b16 v[50:51], v175 offset:2048\n\t" \
  "ds_read_b64_tr_b16 v[52:53], v175 offset:32\n\t" \
  "ds_read_b64_tr_b16 v[54:55], v175 offset:2080\n\t" \
  "ds_read_b64_tr_b16 v[56:57], v175 offset:64\n\t" \
  "ds_read_b64_tr_b16 v[58:59], v175 offset:2112\n\t" \
  "ds_read_b64_tr_b16 v[60:61], v175 offset:96\n\t" \
  "ds_read_b64_tr_b16 v[62:63], v175 offset:2144\n\t" \
  "att_pp1_%=:\n\t" \
  "v_exp_f32 v199, v199\n\t" \
  "v_exp_f32 v200, v200\n\t" \
  "v_exp_f32 v201, v201\n\t" \
  "v_mfma_f32_32x32x8_bf16 v[176:191], v[236:237], v[238:239], v[176:191]\n\t" \
  "v_cvt_pk_bf16_f32 v226, v196, v197\n\t" \
  "v_cvt_pk_bf16_f32 v227, v198, v199\n\t" \
  "v_exp_f32 v202, v202\n\t" \
  "v_exp_f32 v203, v203\n\t" \
  "v_exp_f32 v204, v204\n\t" \
  "v_exp_f32 v205, v205\n\t" \
  "v_mfma_f32_32x32x16_bf16 v[152:167], v[240:243], v[224:227], v[152:167]\n\t" \
  "ds_read_b128 v[240:243], v254 offset:10496\n\t" \
  "v_cvt_pk_bf16_f32 v228, v200, v201\n\t" \
  "v_cvt_pk_bf16_f32 v229, v202, v203\n\t" \
  "v_exp_f32 v206, v206\n\t" \
  "v_exp_f32 v207, v207\n\t" \
  "v_cvt_pk_bf16_f32 v230, v204, v205\n\t" \
  "v_cvt_pk_bf16_f32 v231, v206, v207\n\t" \
  "v_exp_f32 v176, v176\n\t" \
  "v_exp_f32 v177, v177\n\t" \
  "v_mfma_f32_32x32x16_bf16 v[152:167], v[244:247], v[228:231], v[152:167]\n\t" \
  "ds_read_b128 v[244:247], v254 offset:11168\n\t" \
  "v_exp_f32 v178, v178\n\t" \
  "v_exp_f32 v179, v179\n\t" \
  "v_exp_f32 v180, v180\n\t" \
  "v_exp_f32 v181, v181\n\t" \
  "v_mfma_f32_32x32x16_bf16 v[192:207], v[232:235], v[168:171], 0\n\t" \
  "ds_read_b128 v[232:235], v252 offset:15744\n\t" \
  "v_cvt_pk_bf16_f32 v224, v176, v177\n\t" \
  "v_cvt_pk_bf16_f32 v225, v178, v179\n\t" \
  "v_exp_f32 v182, v182\n\t" \
  "v_exp_f32 v183, v183\n\t" \
  "v_exp_f32 v184, v184\n\t" \
  "v_exp_f32 v185, v185\n\t" \
  "v_mfma_f32_32x32x8_bf16 v[192:207], v[236:237], v[172:173], v[192:207]\n\t" \
  "ds_read_b32 v236, v253 offset:15744\n\t" \
  "v_cvt_pk_bf16_f32 v226, v180, v181\n\t" \
  "v_cvt_pk_bf16_f32 v227, v182, v183\n\t" \
  "v_exp_f32 v186, v186\n\t" \
  "v_exp_f32 v187, v187\n\t" \
  "v_exp_f32 v188, v188\n\t" \
  "v_exp_f32 v189, v189\n\t" \
  "s_waitcnt lgkmcnt(3)\n\t" \
  "v_mfma_f32_32x32x16_bf16 v[208:223], v[240:243], v[224:227], v[208:223]\n\t" \
  "v_cvt_pk_bf16_f32 v228, v184, v185\n\t" \
  "v_cvt_pk_bf16_f32 v229, v186, v187\n\t" \
  "v_exp_f32 v190, v190\n\t" \
  "v_exp_f32 v191, v191\n\t" \
  "v_cvt_pk_bf16_f32 v230, v188, v189\n\t" \
  "v_cvt_pk_bf16_f32 v231, v190, v191\n\t" \
  "v_exp_f32 v192, v192\n\t" \
  "v_exp_f32 v193, v193\n\t" \
  "s_waitcnt lgkmcnt(0)\n\t" \
  "v_mfma_f32_32x32x16_bf16 v[208:223], v[244:247], v[228:231], v[208:223]\n\t" \
  "v_exp_f32 v194, v194\n\t" \
  "v_exp_f32 v195, v195\n\t" \
  "v_exp_f32 v196, v196\n\t" \
  "v_exp_f32 v197, v197\n\t" \
  "v_mfma_f32_32x32x16_bf16 v[176:191], v[232:235], v[248:251], 0\n\t" \
  "v_cvt_pk_bf16_f32 v224, v192, v193\n\t" \
  "v_cvt_pk_bf16_f32 v225, v194, v195\n\t" \
  "v_exp_f32 v198, v198\n\t" \
  "v_exp_f32 v199, v199\n\t" \
  "v_exp_f32 v200, v200\n\t" \
  "v_exp_f32 v201, v201\n\t" \
  "v_mfma_f32_32x32x8_bf16 v[176:191], v[236:237], v[238:239], v[176:191]\n\t" \
  "v_cvt_pk_bf16_f32 v226, v196, v197\n\t" \
  "v_cvt_pk_bf16_f32 v227, v198, v199\n\t" \
  "v_exp_f32 v202, v202\n\t" \
  "v_exp_f32 v203, v203\n\t" \
  "v_exp_f32 v204, v204\n\t" \
  "v_exp_f32 v205, v205\n\t" \
  "v_mfma_f32_32x32x16_bf16 v[152:167], v[240:243], v[224:227], v[152:167]\n\t" \
  "ds_read_b128 v[240:243], v254 offset:15744\n\t" \
  "v_cvt_pk_bf16_f32 v228, v200, v201\n\t" \
  "v_cvt_pk_bf16_f32 v229, v202, v203\n\t" \
  "v_exp_f32 v206, v206\n\t" \
  "v_exp_f32 v207, v207\n\t" \
  "v_cvt_pk_bf16_f32 v230, v204, v205\n\t" \
  "v_cvt_pk_bf16_f32 v231, v206, v207\n\t" \
  "s_waitcnt vmcnt(6)\n\t" \
  "s_barrier\n\t" \
  "v_exp_f32 v176, v176\n\t" \
  "v_exp_f32 v177, v177\n\t" \
  "v_mfma_f32_32x32x16_bf16 v[152:167], v[244:247], v[228:231], v[152:167]\n\t" \
  "ds_read_b128 v[244:247], v254 offset:16416\n\t" \
  "v_exp_f32 v178, v178\n\t" \
  "v_exp_f32 v179, v179\n\t" \
  "v_exp_f32 v180, v180\n\t" \
  "v_exp_f32 v181, v181\n\t" \
  "v_mfma_f32_32x32x16_bf16 v[192:207], v[232:235], v[168:171], 0\n\t" \
  "v_add_u32 v252, 0xfffdd680, v252\n\t" \
  "ds_read_b128 v[232:235], v252 offset:0\n\t" \
  "v_cvt_pk_bf16_f32 v224, v176, v177\n\t" \
  "v_cvt_pk_bf16_f32 v225, v178, v179\n\t" \
  "v_exp_f32 v182, v182\n\t" \
  "s_mov_b32 m0, %[kv3m]\n\t" \
  "s_mov_b32 %[t0], %[kv3s]\n\t" \
  "buffer_load_dwordx4 v255, %[srd], %[t0] offen offset:0 sc1 lds\n\t" \
  "v_exp_f32 v183, v183\n\t" \
  "v_exp_f32 v184, v184\n\t" \
  "v_exp_f32 v185, v185\n\t" \
  "v_mfma_f32_32x32x8_bf16 v[192:207], v[236:237], v[172:173], v[192:207]\n\t" \
  "v_add_u32 v253, 0xfffdd680, v253\n\t" \
  "ds_read_b32 v236, v253 offset:0\n\t" \
  "v_cvt_pk_bf16_f32 v226, v180, v181\n\t" \
  "v_cvt_pk_bf16_f32 v227, v182, v183\n\t" \
  "v_exp_f32 v186, v186\n\t" \
  "v_exp_f32 v187, v187\n\t" \
  "v_exp_f32 v188, v188\n\t" \
  "v_exp_f32 v189, v189\n\t" \
  "s_waitcnt lgkmcnt(3)\n\t" \
  "v_mfma_f32_32x32x16_bf16 v[208:223], v[240:243], v[224:227], v[208:223]\n\t" \
  "v_cvt_pk_bf16_f32 v228, v184, v185\n\t" \
  "v_cvt_pk_bf16_f32 v229, v186, v187\n\t" \
  "v_exp_f32 v190, v190\n\t" \
  "buffer_load_dwordx4 v255, %[srd], %[t0] offen offset:1024 sc1 lds\n\t" \
  "v_exp_f32 v191, v191\n\t" \
  "v_cvt_pk_bf16_f32 v230, v188, v189\n\t" \
  "v_cvt_pk_bf16_f32 v231, v190, v191\n\t" \
  "v_exp_f32 v192, v192\n\t" \
  "v_exp_f32 v193, v193\n\t" \
  "s_waitcnt lgkmcnt(0)\n\t" \
  "v_mfma_f32_32x32x16_bf16 v[208:223], v[244:247], v[228:231], v[208:223]\n\t" \
  "v_exp_f32 v194, v194\n\t" \
  "v_exp_f32 v195, v195\n\t" \
  "v_exp_f32 v196, v196\n\t" \
  "v_exp_f32 v197, v197\n\t" \
  "v_mfma_f32_32x32x16_bf16 v[176:191], v[232:235], v[248:251], 0\n\t" \
  "v_cvt_pk_bf16_f32 v224, v192, v193\n\t" \
  "v_cvt_pk_bf16_f32 v225, v194, v195\n\t" \
  "v_exp_f32 v198, v198\n\t" \
  "buffer_load_dwordx4 v255, %[srd], %[t0] offen offset:2048 sc1 lds\n\t" \
  "v_exp_f32 v199, v199\n\t" \
  "v_exp_f32 v200, v200\n\t" \
  "v_exp_f32 v201, v201\n\t" \
  "v_mfma_f32_32x32x8_bf16 v[176:191], v[236:237], v[238:239], v[176:191]\n\t" \
  "v_cvt_pk_bf16_f32 v226, v196, v197\n\t" \
  "v_cvt_pk_bf16_f32 v227, v198, v199\n\t" \
  "v_exp_f32 v202, v202\n\t" \
  "v_exp_f32 v203, v203\n\t" \
  "v_exp_f32 v204, v204\n\t" \
  "v_exp_f32 v205, v205\n\t" \
  "v_mfma_f32_32x32x16_bf16 v[152:167], v[240:243], v[224:227], v[152:167]\n\t" \
  "v_add_u32 v254, 0xfffdd680, v254\n\t" \
  "ds_read_b128 v[240:243], v254 offset:0\n\t" \
  "v_cvt_pk_bf16_f32 v228, v200, v201\n\t" \
  "v_cvt_pk_bf16_f32 v229, v202, v203\n\t" \
  "v_exp_f32 v206, v206\n\t" \
  "buffer_load_dwordx4 v255, %[srd], %[t0] offen offset:3072 sc1 lds\n\t" \
  "v_exp_f32 v207, v207\n\t" \
  "v_cvt_pk_bf16_f32 v230, v204, v205\n\t" \
  "v_cvt_pk_bf16_f32 v231, v206, v207\n\t" \
  "v_exp_f32 v176, v176\n\t" \
  "v_exp_f32 v177, v177\n\t" \
  "v_mfma_f32_32x32x16_bf16 v[152:167], v[244:247], v[228:231], v[152:167]\n\t" \
  "ds_read_b128 v[244:247], v254 offset:672\n\t" \
  "v_exp_f32 v178, v178\n\t" \
  "v_exp_f32 v179, v179\n\t" \
  "v_exp_f32 v180, v180\n\t" \
  "v_exp_f32 v181, v181\n\t" \
  "v_mfma_f32_32x32x16_bf16 v[192:207], v[232:235], v[168:171], 0\n\t" \
  "ds_read_b128 v[232:235], v252 offset:5248\n\t" \
  "v_cvt_pk_bf16_f32 v224, v176, v177\n\t" \
  "v_cvt_pk_bf16_f32 v225, v178, v179\n\t" \
  "v_exp_f32 v182, v182\n\t" \
  "s_cmp_eq_u32 %[ppk], 0\n\t" \
  "s_cbranch_scc1 att_pp2_%=\n\t" \
  "buffer_store_dwordx4 v[48:51], v255, %[srd], %[ppd] offen sc1\n\t" \
  "s_add_u32 %[ppd], %[ppd], %[ppk]\n\t" \
  "buffer_store_dwordx4 v[52:55], v255, %[srd], %[ppd] offen sc1\n\t" \
  "s_add_u32 %[ppd], %[ppd], %[ppk]\n\t" \
  "buffer_store_dwordx4 v[56:59], v255, %[srd], %[ppd] offen sc1\n\t" \
  "s_add_u32 %[ppd], %[ppd], %[ppk]\n\t" \
  "buffer_store_dwordx4 v[60:63], v255, %[srd], %[ppd] offen sc1\n\t" \
  "att_pp2_%=:\n\t" \
  "v_exp_f32 v183, v183\n\t" \
  "v_exp_f32 v184, v184\n\t" \
  "v_exp_f32 v185, v185\n\t" \
  "v_mfma_f32_32x32x8_bf16 v[192:207], v[236:237], v[172:173], v[192:207]\n\t" \
  "ds_read_b32 v236, v253 offset:5248\n\t" \
  "v_cvt_pk_bf16_f32 v226, v180, v181\n\t" \
  "v_cvt_pk_bf16_f32 v227, v182, v183\n\t" \
  "v_exp_f32 v186, v186\n\t" \
  "v_exp_f32 v187, v187\n\t" \
  "v_exp_f32 v188, v188\n\t" \
  "v_exp_f32 v189, v189\n\t" \
  "s_waitcnt lgkmcnt(3)\n\t" \
  "v_mfma_f32_32x32x16_bf16 v[208:223], v[240:243], v[224:227], v[208:223]\n\t" \
  "v_cvt_pk_bf16_f32 v228, v184, v185\n\t" \
  "v_cvt_pk_bf16_f32 v229, v186, v187\n\t" \
  "v_exp_f32 v190, v190\n\t" \
  "v_exp_f32 v191, v191\n\t" \
  "v_cvt_pk_bf16_f32 v230, v188, v189\n\t" \
  "v_cvt_pk_bf16_f32 v231, v190, v191\n\t" \
  "v_exp_f32 v192, v192\n\t" \
  "v_exp_f32 v193, v193\n\t" \
  "s_waitcnt lgkmcnt(0)\n\t" \
  "v_mfma_f32_32x32x16_bf16 v[208:223], v[244:247], v[228:231], v[208:223]\n\t" \
  "v_exp_f32 v194, v194\n\t" \
  "v_exp_f32 v195, v195\n\t" \
  "v_exp_f32 v196, v196\n\t" \
  "v_exp_f32 v197, v197\n\t" \
  "v_mfma_f32_32x32x16_bf16 v[176:191], v[232:235], v[248:251], 0\n\t" \
  "v_cvt_pk_bf16_f32 v224, v192, v193\n\t" \
  "v_cvt_pk_bf16_f32 v225, v194, v195\n\t" \
  "v_exp_f32 v198, v198\n\t" \
  "v_exp_f32 v199, v199\n\t" \
  "v_exp_f32 v200, v200\n\t" \
  "v_exp_f32 v201, v201\n\t" \
  "v_mfma_f32_32x32x8_bf16 v[176:191], v[236:237], v[238:239], v[176:191]\n\t" \
  "v_cvt_pk_bf16_f32 v226, v196, v197\n\t" \
  "v_cvt_pk_bf16_f32 v227, v198, v199\n\t" \
  "v_exp_f32 v202, v202\n\t" \
  "v_exp_f32 v203, v203\n\t" \
  "v_exp_f32 v204, v204\n\t" \
  "v_exp_f32 v205, v205\n\t" \
  "v_mfma_f32_32x32x16_bf16 v[152:167], v[240:243], v[224:227], v[152:167]\n\t" \
  "ds_read_b128 v[240:243], v254 offset:5248\n\t" \
  "v_cvt_pk_bf16_f32 v228, v200, v201\n\t" \
  "v_cvt_pk_bf16_f32 v229, v202, v203\n\t" \
  "v_exp_f32 v206, v206\n\t" \
  "v_exp_f32 v207, v207\n\t" \
  "v_cvt_pk_bf16_f32 v230, v204, v205\n\t" \
  "v_cvt_pk_bf16_f32 v231, v206, v207\n\t" \
  "v_exp_f32 v176, v176\n\t" \
  "v_exp_f32 v177, v177\n\t" \
  "v_mfma_f32_32x32x16_bf16 v[152:167], v[244:247], v[228:231], v[152:167]\n\t" \
  "ds_read_b128 v[244:247], v254 offset:5920\n\t" \
  "v_exp_f32 v178, v178\n\t" \
  "v_exp_f32 v179, v179\n\t" \
  "v_exp_f32 v180, v180\n\t" \
  "v_exp_f32 v181, v181\n\t" \
  "v_mfma_f32_32x32x16_bf16 v[192:207], v[232:235], v[168:171], 0\n\t" \
  "ds_read_b128 v[232:235], v252 offset:10496\n\t" \
  "v_cvt_pk_bf16_f32 v224, v176, v177\n\t" \
  "v_cvt_pk_bf16_f32 v225, v178, v179\n\t" \
  "v_exp_f32 v182, v182\n\t" \
  "v_exp_f32 v183, v183\n\t" \
  "v_exp_f32 v184, v184\n\t" \
  "v_exp_f32 v185, v185\n\t" \
  "v_mfma_f32_32x32x8_bf16 v[192:207], v[236:237], v[172:173], v[192:207]\n\t" \
  "ds_read_b32 v236, v253 offset:10496\n\t" \
  "v_cvt_pk_bf16_f32 v226, v180, v181\n\t" \
  "v_cvt_pk_bf16_f32 v227, v182, v183\n\t" \
  "v_exp_f32 v186, v186\n\t" \
  "v_exp_f32 v187, v187\n\t" \
  "v_exp_f32 v188, v188\n\t" \
  "v_exp_f32 v189, v189\n\t" \
  "s_waitcnt lgkmcnt(3)\n\t" \
  "v_mfma_f32_32x32x16_bf16 v[208:223], v[240:243], v[224:227], v[208:223]\n\t" \
  "v_cvt_pk_bf16_f32 v228, v184, v185\n\t" \
  "v_cvt_pk_bf16_f32 v229, v186, v187\n\t" \
  "v_exp_f32 v190, v190\n\t" \
  "v_exp_f32 v191, v191\n\t" \
  "v_cvt_pk_bf16_f32 v230, v188, v189\n\t" \
  "v_cvt_pk_bf16_f32 v231, v190, v191\n\t" \
  "v_exp_f32 v192, v192\n\t" \
  "v_exp_f32 v193, v193\n\t" \
  "s_waitcnt lgkmcnt(0)\n\t" \
  "v_mfma_f32_32x32x16_bf16 v[208:223], v[244:247], v[228:231], v[208:223]\n\t" \
  "v_exp_f32 v194, v194\n\t" \
  "v_exp_f32 v195, v195\n\t" \
  "v_exp_f32 v196, v196\n\t" \
  "v_exp_f32 v197, v197\n\t" \
  "v_mfma_f32_32x32x16_bf16 v[176:191], v[232:235], v[248:251], 0\n\t" \
  "v_cvt_pk_bf16_f32 v224, v192, v193\n\t" \
  "v_cvt_pk_bf16_f32 v225, v194, v195\n\t" \
  "v_exp_f32 v198, v198\n\t" \
  "v_exp_f32 v199, v199\n\t" \
  "v_exp_f32 v200, v200\n\t" \
  "v_exp_f32 v201, v201\n\t" \
  "v_mfma_f32_32x32x8_bf16 v[176:191], v[236:237], v[238:239], v[176:191]\n\t" \
  "v_cvt_pk_bf16_f32 v226, v196, v197\n\t" \
  "v_cvt_pk_bf16_f32 v227, v198, v199\n\t" \
  "v_exp_f32 v202, v202\n\t" \
  "v_exp_f32 v203, v203\n\t" \
  "v_exp_f32 v204, v204\n\t" \
  "v_exp_f32 v205, v205\n\t" \
  "v_mfma_f32_32x32x16_bf16 v[152:167], v[240:243], v[224:227], v[152:167]\n\t" \
  "ds_read_b128 v[240:243], v254 offset:10496\n\t" \
  "v_cvt_pk_bf16_f32 v228, v200, v201\n\t" \
  "v_cvt_pk_bf16_f32 v229, v202, v203\n\t" \
  "v_exp_f32 v206, v206\n\t" \
  "v_exp_f32 v207, v207\n\t" \
  "v_cvt_pk_bf16_f32 v230, v204, v205\n\t" \
  "v_cvt_pk_bf16_f32 v231, v206, v207\n\t" \
  "v_exp_f32 v176, v176\n\t" \
  "v_exp_f32 v177, v177\n\t" \
  "v_mfma_f32_32x32x16_bf16 v[152:167], v[244:247], v[228:231], v[152:167]\n\t" \
  "ds_read_b128 v[244:247], v254 offset:11168\n\t" \
  "v_exp_f32 v178, v178\n\t" \
  "v_exp_f32 v179, v179\n\t" \
  "v_exp_f32 v180, v180\n\t" \
  "v_exp_f32 v181, v181\n\t" \
  "v_mfma_f32_32x32x16_bf16 v[192:207], v[232:235], v[168:171], 0\n\t" \
  "ds_read_b128 v[232:235], v252 offset:15744\n\t" \
  "v_cvt_pk_bf16_f32 v224, v176, v177\n\t" \
  "v_cvt_pk_bf16_f32 v225, v178, v179\n\t" \
  "v_exp_f32 v182, v182\n\t" \
  "v_exp_f32 v183, v183\n\t" \
  "v_exp_f32 v184, v184\n\t" \
  "v_exp_f32 v185, v185\n\t" \
  "v_mfma_f32_32x32x8_bf16 v[192:207], v[236:237], v[172:173], v[192:207]\n\t" \
  "ds_read_b32 v236, v253 offset:15744\n\t" \
  "v_cvt_pk_bf16_f32 v226, v180, v181\n\t" \
  "v_cvt_pk_bf16_f32 v227, v182, v183\n\t" \
  "v_exp_f32 v186, v186\n\t" \
  "v_exp_f32 v187, v187\n\t" \
  "v_exp_f32 v188, v188\n\t" \
  "v_exp_f32 v189, v189\n\t" \
  "s_waitcnt lgkmcnt(3)\n\t" \
  "v_mfma_f32_32x32x16_bf16 v[208:223], v[240:243], v[224:227], v[208:223]\n\t" \
  "v_cvt_pk_bf16_f32 v228, v184, v185\n\t" \
  "v_cvt_pk_bf16_f32 v229, v186, v187\n\t" \
  "v_exp_f32 v190, v190\n\t" \
  "v_exp_f32 v191, v191\n\t" \
  "v_cvt_pk_bf16_f32 v230, v188, v189\n\t" \
  "v_cvt_pk_bf16_f32 v231, v190, v191\n\t" \
  "v_exp_f32 v192, v192\n\t" \
  "v_exp_f32 v193, v193\n\t" \
  "s_waitcnt lgkmcnt(0)\n\t" \
  "v_mfma_f32_32x32x16_bf16 v[208:223], v[244:247], v[228:231], v[208:223]\n\t" \
  "v_exp_f32 v194, v194\n\t" \
  "v_exp_f32 v195, v195\n\t" \
  "v_exp_f32 v196, v196\n\t" \
  "v_exp_f32 v197, v197\n\t" \
  "v_mfma_f32_32x32x16_bf16 v[176:191], v[232:235], v[248:251], 0\n\t" \
  "v_cvt_pk_bf16_f32 v224, v192, v193\n\t" \
  "v_cvt_pk_bf16_f32 v225, v194, v195\n\t" \
  "v_exp_f32 v198, v198\n\t" \
  "v_exp_f32 v199, v199\n\t" \
  "v_exp_f32 v200, v200\n\t" \
  "v_exp_f32 v201, v201\n\t" \
  "v_mfma_f32_32x32x8_bf16 v[176:191], v[236:237], v[238:239], v[176:191]\n\t" \
  "v_cvt_pk_bf16_f32 v226, v196, v197\n\t" \
  "v_cvt_pk_bf16_f32 v227, v198, v199\n\t" \
  "v_exp_f32 v202, v202\n\t" \
  "v_exp_f32 v203, v203\n\t" \
  "v_exp_f32 v204, v204\n\t" \
  "v_exp_f32 v205, v205\n\t" \
  "v_mfma_f32_32x32x16_bf16 v[152:167], v[240:243], v[224:227], v[152:167]\n\t" \
  "ds_read_b128 v[240:243], v254 offset:15744\n\t" \
  "v_cvt_pk_bf16_f32 v228, v200, v201\n\t" \
  "v_cvt_pk_bf16_f32 v229, v202, v203\n\t" \
  "v_exp_f32 v206, v206\n\t" \
  "v_exp_f32 v207, v207\n\t" \
  "v_cvt_pk_bf16_f32 v230, v204, v205\n\t" \
  "v_cvt_pk_bf16_f32 v231, v206, v207\n\t" \
  "v_exp_f32 v176, v176\n\t" \
  "v_exp_f32 v177, v177\n\t" \
  "v_mfma_f32_32x32x16_bf16 v[152:167], v[244:247], v[228:231], v[152:167]\n\t" \
  "ds_read_b128 v[244:247], v254 offset:16416\n\t" \
  "v_exp_f32 v178, v178\n\t" \
  "v_exp_f32 v179, v179\n\t" \
  "v_exp_f32 v180, v180\n\t" \
  "v_exp_f32 v181, v181\n\t" \
  "v_mfma_f32_32x32x16_bf16 v[192:207], v[232:235], v[168:171], 0\n\t" \
  "v_add_u32 v252, 0x5200, v252\n\t" \
  "ds_read_b128 v[232:235], v252 offset:0\n\t" \
  "v_cvt_pk_bf16_f32 v224, v176, v177\n\t" \
  "v_cvt_pk_bf16_f32 v225, v178, v179\n\t" \
  "v_exp_f32 v182, v182\n\t" \
  "v_exp_f32 v183, v183\n\t" \
  "v_exp_f32 v184, v184\n\t" \
  "v_exp_f32 v185, v185\n\t" \
  "v_mfma_f32_32x32x8_bf16 v[192:207], v[236:237], v[172:173], v[192:207]\n\t" \
  "v_add_u32 v253, 0x5200, v253\n\t" \
  "ds_read_b32 v236, v253 offset:0\n\t" \
  "v_cvt_pk_bf16_f32 v226, v180, v181\n\t" \
  "v_cvt_pk_bf16_f32 v227, v182, v183\n\t" \
  "v_exp_f32 v186, v186\n\t" \
  "v_exp_f32 v187, v187\n\t" \
  "v_exp_f32 v188, v188\n\t" \
  "v_exp_f32 v189, v189\n\t" \
  "s_waitcnt lgkmcnt(3)\n\t" \
  "v_mfma_f32_32x32x16_bf16 v[208:223], v[240:243], v[224:227], v[208:223]\n\t" \
  "v_cvt_pk_bf16_f32 v228, v184, v185\n\t" \
  "v_cvt_pk_bf16_f32 v229, v186, v187\n\t" \
  "v_exp_f32 v190, v190\n\t" \
  "v_exp_f32 v191, v191\n\t" \
  "v_cvt_pk_bf16_f32 v230, v188, v189\n\t" \
  "v_cvt_pk_bf16_f32 v231, v190, v191\n\t" \
  "v_exp_f32 v192, v192\n\t" \
  "v_exp_f32 v193, v193\n\t" \
  "s_waitcnt lgkmcnt(0)\n\t" \
  "v_mfma_f32_32x32x16_bf16 v[208:223], v[244:247], v[228:231], v[208:223]\n\t" \
  "v_exp_f32 v194, v194\n\t" \
  "v_exp_f32 v195, v195\n\t" \
  "v_exp_f32 v196, v196\n\t" \
  "v_exp_f32 v197, v197\n\t" \
  "v_mfma_f32_32x32x16_bf16 v[176:191], v[232:235], v[248:251], 0\n\t" \
  "v_cvt_pk_bf16_f32 v224, v192, v193\n\t" \
  "v_cvt_pk_bf16_f32 v225, v194, v195\n\t" \
  "v_exp_f32 v198, v198\n\t" \
  "v_exp_f32 v199, v199\n\t" \
  "v_exp_f32 v200, v200\n\t" \
  "v_exp_f32 v201, v201\n\t" \
  "v_mfma_f32_32x32x8_bf16 v[176:191], v[236:237], v[238:239], v[176:191]\n\t" \
  "v_cvt_pk_bf16_f32 v226, v196, v197\n\t" \
  "v_cvt_pk_bf16_f32 v227, v198, v199\n\t" \
  "v_exp_f32 v202, v202\n\t" \
  "v_exp_f32 v203, v203\n\t" \
  "v_exp_f32 v204, v204\n\t" \
  "v_exp_f32 v205, v205\n\t" \
  "v_mfma_f32_32x32x16_bf16 v[152:167], v[240:243], v[224:227], v[152:167]\n\t" \
  "v_add_u32 v254, 0x5200, v254\n\t" \
  "ds_read_b128 v[240:243], v254 offset:0\n\t" \
  "v_cvt_pk_bf16_f32 v228, v200, v201\n\t" \
  "v_cvt_pk_bf16_f32 v229, v202, v203\n\t" \
  "v_exp_f32 v206, v206\n\t" \
  "v_exp_f32 v207, v207\n\t" \
  "v_cvt_pk_bf16_f32 v230, v204, v205\n\t" \
  "v_cvt_pk_bf16_f32 v231, v206, v207\n\t" \
  "v_exp_f32 v176, v176\n\t" \
  "v_exp_f32 v177, v177\n\t" \
  "v_mfma_f32_32x32x16_bf16 v[152:167], v[244:247], v[228:231], v[152:167]\n\t" \
  "ds_read_b128 v[244:247], v254 offset:672\n\t" \
  "v_exp_f32 v178, v178\n\t" \
  "v_exp_f32 v179, v179\n\t" \
  "v_exp_f32 v180, v180\n\t" \
  "v_exp_f32 v181, v181\n\t" \
  "v_mfma_f32_32x32x16_bf16 v[192:207], v[232:235], v[168:171], 0\n\t" \
  "ds_read_b128 v[232:235], v252 offset:5248\n\t" \
  "v_cvt_pk_bf16_f32 v224, v176, v177\n\t" \
  "v_cvt_pk_bf16_f32 v225, v178, v179\n\t" \
  "v_exp_f32 v182, v182\n\t" \
  "v_exp_f32 v183, v183\n\t" \
  "v_exp_f32 v184, v184\n\t" \
  "v_exp_f32 v185, v185\n\t" \
  "v_mfma_f32_32x32x8_bf16 v[192:207], v[236:237], v[172:173], v[192:207]\n\t" \
  "ds_read_b32 v236, v253 offset:5248\n\t" \
  "v_cvt_pk_bf16_f32 v226, v180, v181\n\t" \
  "v_cvt_pk_bf16_f32 v227, v182, v183\n\t" \
  "v_exp_f32 v186, v186\n\t" \
  "v_exp_f32 v187, v187\n\t" \
  "v_exp_f32 v188, v188\n\t" \
  "v_exp_f32 v189, v189\n\t" \
  "s_waitcnt lgkmcnt(3)\n\t" \
  "v_mfma_f32_32x32x16_bf16 v[208:223], v[240:243], v[224:227], v[208:223]\n\t" \
  "v_cvt_pk_bf16_f32 v228, v184, v185\n\t" \
  "v_cvt_pk_bf16_f32 v229, v186, v187\n\t" \
  "v_exp_f32 v190, v190\n\t" \
  "v_exp_f32 v191, v191\n\t" \
  "v_cvt_pk_bf16_f32 v230, v188, v189\n\t" \
  "v_cvt_pk_bf16_f32 v231, v190, v191\n\t" \
  "v_exp_f32 v192, v192\n\t" \
  "v_exp_f32 v193, v193\n\t" \
  "s_waitcnt lgkmcnt(0)\n\t" \
  "v_mfma_f32_32x32x16_bf16 v[208:223], v[244:247], v[228:231], v[208:223]\n\t" \
  "v_exp_f32 v194, v194\n\t" \
  "v_exp_f32 v195, v195\n\t" \
  "v_exp_f32 v196, v196\n\t" \
  "v_exp_f32 v197, v197\n\t" \
  "v_mfma_f32_32x32x16_bf16 v[176:191], v[232:235], v[248:251], 0\n\t" \
  "v_cvt_pk_bf16_f32 v224, v192, v193\n\t" \
  "v_cvt_pk_bf16_f32 v225, v194, v195\n\t" \
  "v_exp_f32 v198, v198\n\t" \
  "v_exp_f32 v199, v199\n\t" \
  "v_exp_f32 v200, v200\n\t" \
  "v_exp_f32 v201, v201\n\t" \
  "v_mfma_f32_32x32x8_bf16 v[176:191], v[236:237], v[238:239], v[176:191]\n\t" \
  "v_cvt_pk_bf16_f32 v226, v196, v197\n\t" \
  "v_cvt_pk_bf16_f32 v227, v198, v199\n\t" \
  "v_exp_f32 v202, v202\n\t" \
  "v_exp_f32 v203, v203\n\t" \
  "v_exp_f32 v204, v204\n\t" \
  "v_exp_f32 v205, v205\n\t" \
  "v_mfma_f32_32x32x16_bf16 v[152:167], v[240:243], v[224:227], v[152:167]\n\t" \
  "ds_read_b128 v[240:243], v254 offset:5248\n\t" \
  "v_cvt_pk_bf16_f32 v228, v200, v201\n\t" \
  "v_cvt_pk_bf16_f32 v229, v202, v203\n\t" \
  "v_exp_f32 v206, v206\n\t" \
  "v_exp_f32 v207, v207\n\t" \
  "v_cvt_pk_bf16_f32 v230, v204, v205\n\t" \
  "v_cvt_pk_bf16_f32 v231, v206, v207\n\t" \
  "s_waitcnt vmcnt(0)\n\t" \
  "s_barrier\n\t" \
  "s_cmp_lg_u32 %[wid], 0\n\t" \
  "s_cbranch_scc1 att_nf_%=\n\t" \
  "v_mov_b32 v32, %[tlo]\n\t" \
  "v_mov_b32 v33, %[thi]\n\t" \
  "v_lshlrev_b32 v34, 7, v255\n\t" \
  "s_mov_b64 exec, 0xff\n\t" \
  "buffer_store_dwordx2 v[32:33], v34, %[srd], %[fws] offen sc1\n\t" \
  "s_mov_b64 exec, -1\n\t" \
  "att_nf_%=:\n\t" \
  "v_exp_f32 v176, v176\n\t" \
  "v_exp_f32 v177, v177\n\t" \
  "v_mfma_f32_32x32x16_bf16 v[152:167], v[244:247], v[228:231], v[152:167]\n\t" \
  "ds_read_b128 v[244:247], v254 offset:5920\n\t" \
  "v_exp_f32 v178, v178\n\t" \
  "v_exp_f32 v179, v179\n\t" \
  "v_exp_f32 v180, v180\n\t" \
  "v_exp_f32 v181, v181\n\t" \
  "v_mfma_f32_32x32x16_bf16 v[192:207], v[232:235], v[168:171], 0\n\t" \
  "ds_read_b128 v[232:235], v252 offset:10496\n\t" \
  "v_cvt_pk_bf16_f32 v224, v176, v177\n\t" \
  "v_cvt_pk_bf16_f32 v225, v178, v179\n\t" \
  "v_exp_f32 v182, v182\n\t" \
  "v_exp_f32 v183, v183\n\t" \
  "v_exp_f32 v184, v184\n\t" \
  "v_exp_f32 v185, v185\n\t" \
  "v_mfma_f32_32x32x8_bf16 v[192:207], v[236:237], v[172:173], v[192:207]\n\t" \
  "ds_read_b32 v236, v253 offset:10496\n\t" \
  "v_cvt_pk_bf16_f32 v226, v180, v181\n\t" \
  "v_cvt_pk_bf16_f32 v227, v182, v183\n\t" \
  "v_exp_f32 v186, v186\n\t" \
  "v_exp_f32 v187, v187\n\t" \
  "v_exp_f32 v188, v188\n\t" \
  "v_exp_f32 v189, v189\n\t" \
  "s_waitcnt lgkmcnt(3)\n\t" \
  "v_mfma_f32_32x32x16_bf16 v[208:223], v[240:243], v[224:227], v[208:223]\n\t" \
  "v_cvt_pk_bf16_f32 v228, v184, v185\n\t" \
  "v_cvt_pk_bf16_f32 v229, v186, v187\n\t" \
  "v_exp_f32 v190, v190\n\t" \
  "v_exp_f32 v191, v191\n\t" \
  "v_cvt_pk_bf16_f32 v230, v188, v189\n\t" \
  "v_cvt_pk_bf16_f32 v231, v190, v191\n\t" \
  "v_exp_f32 v192, v192\n\t" \
  "v_exp_f32 v193, v193\n\t" \
  "s_waitcnt lgkmcnt(0)\n\t" \
  "v_mfma_f32_32x32x16_bf16 v[208:223], v[244:247], v[228:231], v[208:223]\n\t" \
  "v_exp_f32 v194, v194\n\t" \
  "v_exp_f32 v195, v195\n\t" \
  "v_exp_f32 v196, v196\n\t" \
  "v_exp_f32 v197, v197\n\t" \
  "v_mfma_f32_32x32x16_bf16 v[176:191], v[232:235], v[248:251], 0\n\t" \
  "v_cvt_pk_bf16_f32 v224, v192, v193\n\t" \
  "v_cvt_pk_bf16_f32 v225, v194, v195\n\t" \
  "v_exp_f32 v198, v198\n\t" \
  "v_exp_f32 v199, v199\n\t" \
  "v_exp_f32 v200, v200\n\t" \
  "v_exp_f32 v201, v201\n\t" \
  "v_mfma_f32_32x32x8_bf16 v[176:191], v[236:237], v[238:239], v[176:191]\n\t" \
  "v_cvt_pk_bf16_f32 v226, v196, v197\n\t" \
  "v_cvt_pk_bf16_f32 v227, v198, v199\n\t" \
  "v_exp_f32 v202, v202\n\t" \
  "v_exp_f32 v203, v203\n\t" \
  "v_exp_f32 v204, v204\n\t" \
  "v_exp_f32 v205, v205\n\t" \
  "v_mfma_f32_32x32x16_bf16 v[152:167], v[240:243], v[224:227], v[152:167]\n\t" \
  "ds_read_b128 v[240:243], v254 offset:10496\n\t" \
  "v_cvt_pk_bf16_f32 v228, v200, v201\n\t" \
  "v_cvt_pk_bf16_f32 v229, v202, v203\n\t" \
  "v_exp_f32 v206, v206\n\t" \
  "v_exp_f32 v207, v207\n\t" \
  "v_cvt_pk_bf16_f32 v230, v204, v205\n\t" \
  "v_cvt_pk_bf16_f32 v231, v206, v207\n\t" \
  "v_exp_f32 v176, v176\n\t" \
  "v_exp_f32 v177, v177\n\t" \
  "v_mfma_f32_32x32x16_bf16 v[152:167], v[244:247], v[228:231], v[152:167]\n\t" \
  "ds_read_b128 v[244:247], v254 offset:11168\n\t" \
  "v_exp_f32 v178, v178\n\t" \
  "v_exp_f32 v179, v179\n\t" \
  "v_exp_f32 v180, v180\n\t" \
  "v_exp_f32 v181, v181\n\t" \
  "v_mfma_f32_32x32x16_bf16 v[192:207], v[232:235], v[168:171], 0\n\t" \
  "ds_read_b128 v[232:235], v252 offset:15744\n\t" \
  "v_cvt_pk_bf16_f32 v224, v176, v177\n\t" \
  "v_cvt_pk_bf16_f32 v225, v178, v179\n\t" \
  "v_exp_f32 v182, v182\n\t" \
  "v_exp_f32 v183, v183\n\t" \
  "v_exp_f32 v184, v184\n\t" \
  "v_exp_f32 v185, v185\n\t" \
  "v_mfma_f32_32x32x8_bf16 v[192:207], v[236:237], v[172:173], v[192:207]\n\t" \
  "ds_read_b32 v236, v253 offset:15744\n\t" \
  "v_cvt_pk_bf16_f32 v226, v180, v181\n\t" \
  "v_cvt_pk_bf16_f32 v227, v182, v183\n\t" \
  "v_exp_f32 v186, v186\n\t" \
  "v_exp_f32 v187, v187\n\t" \
  "v_exp_f32 v188, v188\n\t" \
  "v_exp_f32 v189, v189\n\t" \
  "s_waitcnt lgkmcnt(3)\n\t" \
  "v_mfma_f32_32x32x16_bf16 v[208:223], v[240:243], v[224:227], v[208:223]\n\t" \
  "v_cvt_pk_bf16_f32 v228, v184, v185\n\t" \
  "v_cvt_pk_bf16_f32 v229, v186, v187\n\t" \
  "v_exp_f32 v190, v190\n\t" \
  "v_exp_f32 v191, v191\n\t" \
  "v_cvt_pk_bf16_f32 v230, v188, v189\n\t" \
  "v_cvt_pk_bf16_f32 v231, v190, v191\n\t" \
  "v_exp_f32 v192, v192\n\t" \
  "v_exp_f32 v193, v193\n\t" \
  "s_waitcnt lgkmcnt(0)\n\t" \
  "v_mfma_f32_32x32x16_bf16 v[208:223], v[244:247], v[228:231], v[208:223]\n\t" \
  "v_exp_f32 v194, v194\n\t" \
  "v_exp_f32 v195, v195\n\t" \
  "v_exp_f32 v196, v196\n\t" \
  "v_exp_f32 v197, v197\n\t" \
  "v_mfma_f32_32x32x16_bf16 v[176:191], v[232:235], v[248:251], 0\n\t" \
  "v_cvt_pk_bf16_f32 v224, v192, v193\n\t" \
  "v_cvt_pk_bf16_f32 v225, v194, v195\n\t" \
  "v_exp_f32 v198, v198\n\t" \
  "v_exp_f32 v199, v199\n\t" \
  "v_exp_f32 v200, v200\n\t" \
  "v_exp_f32 v201, v201\n\t" \
  "v_mfma_f32_32x32x8_bf16 v[176:191], v[236:237], v[238:239], v[176:191]\n\t" \
  "v_cvt_pk_bf16_f32 v226, v196, v197\n\t" \
  "v_cvt_pk_bf16_f32 v227, v198, v199\n\t" \
  "v_exp_f32 v202, v202\n\t" \
  "v_exp_f32 v203, v203\n\t" \
  "v_exp_f32 v204, v204\n\t" \
  "v_exp_f32 v205, v205\n\t" \
  "v_mfma_f32_32x32x16_bf16 v[152:167], v[240:243], v[224:227], v[152:167]\n\t" \
  "ds_read_b128 v[240:243], v254 offset:15744\n\t" \
  "v_cvt_pk_bf16_f32 v228, v200, v201\n\t" \
  "v_cvt_pk_bf16_f32 v229, v202, v203\n\t" \
  "v_exp_f32 v206, v206\n\t" \
  "v_exp_f32 v207, v207\n\t" \
  "v_cvt_pk_bf16_f32 v230, v204, v205\n\t" \
  "v_cvt_pk_bf16_f32 v231, v206, v207\n\t" \
  "v_exp_f32 v176, v176\n\t" \
  "v_exp_f32 v177, v177\n\t" \
  "v_mfma_f32_32x32x16_bf16 v[152:167], v[244:247], v[228:231], v[152:167]\n\t" \
  "ds_read_b128 v[244:247], v254 offset:16416\n\t" \
  "v_exp_f32 v178, v178\n\t" \
  "v_exp_f32 v179, v179\n\t" \
  "v_exp_f32 v180, v180\n\t" \
  "v_exp_f32 v181, v181\n\t" \
  "v_mfma_f32_32x32x16_bf16 v[192:207], v[232:235], v[168:171], 0\n\t" \
  "v_add_u32 v252, 0x5200, v252\n\t" \
  "ds_read_b128 v[232:235], v252 offset:0\n\t" \
  "v_cvt_pk_bf16_f32 v224, v176, v177\n\t" \
  "v_cvt_pk_bf16_f32 v225, v178, v179\n\t" \
  "v_exp_f32 v182, v182\n\t" \
  "v_exp_f32 v183, v183\n\t" \
  "v_exp_f32 v184, v184\n\t" \
  "v_exp_f32 v185, v185\n\t" \
  "v_mfma_f32_32x32x8_bf16 v[192:207], v[236:237], v[172:173], v[192:207]\n\t" \
  "v_add_u32 v253, 0x5200, v253\n\t" \
  "ds_read_b32 v236, v253 offset:0\n\t" \
  "v_cvt_pk_bf16_f32 v226, v180, v181\n\t" \
  "v_cvt_pk_bf16_f32 v227, v182, v183\n\t" \
  "v_exp_f32 v186, v186\n\t" \
  "v_exp_f32 v187, v187\n\t" \
  "v_exp_f32 v188, v188\n\t" \
  "v_exp_f32 v189, v189\n\t" \
  "s_waitcnt lgkmcnt(3)\n\t" \
  "v_mfma_f32_32x32x16_bf16 v[208:223], v[240:243], v[224:227], v[208:223]\n\t" \
  "v_cvt_pk_bf16_f32 v228, v184, v185\n\t" \
  "v_cvt_pk_bf16_f32 v229, v186, v187\n\t" \
  "v_exp_f32 v190, v190\n\t" \
  "v_exp_f32 v191, v191\n\t" \
  "v_cvt_pk_bf16_f32 v230, v188, v189\n\t" \
  "v_cvt_pk_bf16_f32 v231, v190, v191\n\t" \
  "v_exp_f32 v192, v192\n\t" \
  "v_exp_f32 v193, v193\n\t" \
  "s_waitcnt lgkmcnt(0)\n\t" \
  "v_mfma_f32_32x32x16_bf16 v[208:223], v[244:247], v[228:231], v[208:223]\n\t" \
  "v_exp_f32 v194, v194\n\t" \
  "v_exp_f32 v195, v195\n\t" \
  "v_exp_f32 v196, v196\n\t" \
  "v_exp_f32 v197, v197\n\t" \
  "v_mfma_f32_32x32x16_bf16 v[176:191], v[232:235], v[248:251], 0\n\t" \
  "v_cvt_pk_bf16_f32 v224, v192, v193\n\t" \
  "v_cvt_pk_bf16_f32 v225, v194, v195\n\t" \
  "v_exp_f32 v198, v198\n\t" \
  "v_exp_f32 v199, v199\n\t" \
  "v_exp_f32 v200, v200\n\t" \
  "v_exp_f32 v201, v201\n\t" \
  "v_mfma_f32_32x32x8_bf16 v[176:191], v[236:237], v[238:239], v[176:191]\n\t" \
  "v_cvt_pk_bf16_f32 v226, v196, v197\n\t" \
  "v_cvt_pk_bf16_f32 v227, v198, v199\n\t" \
  "v_exp_f32 v202, v202\n\t" \
  "v_exp_f32 v203, v203\n\t" \
  "v_exp_f32 v204, v204\n\t" \
  "v_exp_f32 v205, v205\n\t" \
  "v_mfma_f32_32x32x16_bf16 v[152:167], v[240:243], v[224:227], v[152:167]\n\t" \
  "v_add_u32 v254, 0x5200, v254\n\t" \
  "ds_read_b128 v[240:243], v254 offset:0\n\t" \
  "v_cvt_pk_bf16_f32 v228, v200, v201\n\t" \
  "v_cvt_pk_bf16_f32 v229, v202, v203\n\t" \
  "v_exp_f32 v206, v206\n\t" \
  "v_exp_f32 v207, v207\n\t" \
  "v_cvt_pk_bf16_f32 v230, v204, v205\n\t" \
  "v_cvt_pk_bf16_f32 v231, v206, v207\n\t" \
  "v_exp_f32 v176, v176\n\t" \
  "v_exp_f32 v177, v177\n\t" \
  "v_mfma_f32_32x32x16_bf16 v[152:167], v[244:247], v[228:231], v[152:167]\n\t" \
  "ds_read_b128 v[244:247], v254 offset:672\n\t" \
  "v_exp_f32 v178, v178\n\t" \
  "v_exp_f32 v179, v179\n\t" \
  "v_exp_f32 v180, v180\n\t" \
  "v_exp_f32 v181, v181\n\t" \
  "v_mfma_f32_32x32x16_bf16 v[192:207], v[232:235], v[168:171], 0\n\t" \
  "ds_read_b128 v[232:235], v252 offset:5248\n\t" \
  "v_cvt_pk_bf16_f32 v224, v176, v177\n\t" \
  "v_cvt_pk_bf16_f32 v225, v178, v179\n\t" \
  "v_exp_f32 v182, v182\n\t" \
  "v_exp_f32 v183, v183\n\t" \
  "v_exp_f32 v184, v184\n\t" \
  "v_exp_f32 v185, v185\n\t" \
  "v_mfma_f32_32x32x8_bf16 v[192:207], v[236:237], v[172:173], v[192:207]\n\t" \
  "ds_read_b32 v236, v253 offset:5248\n\t" \
  "v_cvt_pk_bf16_f32 v226, v180, v181\n\t" \
  "v_cvt_pk_bf16_f32 v227, v182, v183\n\t" \
  "v_exp_f32 v186, v186\n\t" \
  "v_exp_f32 v187, v187\n\t" \
  "v_exp_f32 v188, v188\n\t" \
  "v_exp_f32 v189, v189\n\t" \
  "s_waitcnt lgkmcnt(3)\n\t" \
  "v_mfma_f32_32x32x16_bf16 v[208:223], v[240:243], v[224:227], v[208:223]\n\t" \
  "v_cvt_pk_bf16_f32 v228, v184, v185\n\t" \
  "v_cvt_pk_bf16_f32 v229, v186, v187\n\t" \
  "v_exp_f32 v190, v190\n\t" \
  "v_exp_f32 v191, v191\n\t" \
  "v_cvt_pk_bf16_f32 v230, v188, v189\n\t" \
  "v_cvt_pk_bf16_f32 v231, v190, v191\n\t" \
  "v_exp_f32 v192, v192\n\t" \
  "v_exp_f32 v193, v193\n\t" \
  "s_waitcnt lgkmcnt(0)\n\t" \
  "v_mfma_f32_32x32x16_bf16 v[208:223], v[244:247], v[228:231], v[208:223]\n\t" \
  "v_exp_f32 v194, v194\n\t" \
  "v_exp_f32 v195, v195\n\t" \
  "v_exp_f32 v196, v196\n\t" \
  "v_exp_f32 v197, v197\n\t" \
  "v_mfma_f32_32x32x16_bf16 v[176:191], v[232:235], v[248:251], 0\n\t" \
  "v_cvt_pk_bf16_f32 v224, v192, v193\n\t" \
  "v_cvt_pk_bf16_f32 v225, v194, v195\n\t" \
  "v_exp_f32 v198, v198\n\t" \
  "v_exp_f32 v199, v199\n\t" \
  "v_exp_f32 v200, v200\n\t" \
  "v_exp_f32 v201, v201\n\t" \
  "v_mfma_f32_32x32x8_bf16 v[176:191], v[236:237], v[238:239], v[176:191]\n\t" \
  "v_cvt_pk_bf16_f32 v226, v196, v197\n\t" \
  "v_cvt_pk_bf16_f32 v227, v198, v199\n\t" \
  "v_exp_f32 v202, v202\n\t" \
  "v_exp_f32 v203, v203\n\t" \
  "v_exp_f32 v204, v204\n\t" \
  "v_exp_f32 v205, v205\n\t" \
  "v_mfma_f32_32x32x16_bf16 v[152:167], v[240:243], v[224:227], v[152:167]\n\t" \
  "ds_read_b128 v[240:243], v254 offset:5248\n\t" \
  "v_cvt_pk_bf16_f32 v228, v200, v201\n\t" \
  "v_cvt_pk_bf16_f32 v229, v202, v203\n\t" \
  "v_exp_f32 v206, v206\n\t" \
  "v_exp_f32 v207, v207\n\t" \
  "v_cvt_pk_bf16_f32 v230, v204, v205\n\t" \
  "v_cvt_pk_bf16_f32 v231, v206, v207\n\t" \
  "v_exp_f32 v176, v176\n\t" \
  "v_exp_f32 v177, v177\n\t" \
  "v_mfma_f32_32x32x16_bf16 v[152:167], v[244:247], v[228:231], v[152:167]\n\t" \
  "ds_read_b128 v[244:247], v254 offset:5920\n\t" \
  "v_exp_f32 v178, v178\n\t" \
  "v_exp_f32 v179, v179\n\t" \
  "v_exp_f32 v180, v180\n\t" \
  "v_exp_f32 v181, v181\n\t" \
  "v_mfma_f32_32x32x16_bf16 v[192:207], v[232:235], v[168:171], 0\n\t" \
  "ds_read_b128 v[232:235], v252 offset:10496\n\t" \
  "v_cvt_pk_bf16_f32 v224, v176, v177\n\t" \
  "v_cvt_pk_bf16_f32 v225, v178, v179\n\t" \
  "v_exp_f32 v182, v182\n\t" \
  "v_exp_f32 v183, v183\n\t" \
  "v_exp_f32 v184, v184\n\t" \
  "v_exp_f32 v185, v185\n\t" \
  "v_mfma_f32_32x32x8_bf16 v[192:207], v[236:237], v[172:173], v[192:207]\n\t" \
  "ds_read_b32 v236, v253 offset:10496\n\t" \
  "v_cvt_pk_bf16_f32 v226, v180, v181\n\t" \
  "v_cvt_pk_bf16_f32 v227, v182, v183\n\t" \
  "v_exp_f32 v186, v186\n\t" \
  "v_exp_f32 v187, v187\n\t" \
  "v_exp_f32 v188, v188\n\t" \
  "v_exp_f32 v189, v189\n\t" \
  "s_waitcnt lgkmcnt(3)\n\t" \
  "v_mfma_f32_32x32x16_bf16 v[208:223], v[240:243], v[224:227], v[208:223]\n\t" \
  "v_cvt_pk_bf16_f32 v228, v184, v185\n\t" \
  "v_cvt_pk_bf16_f32 v229, v186, v187\n\t" \
  "v_exp_f32 v190, v190\n\t" \
  "v_exp_f32 v191, v191\n\t" \
  "v_cvt_pk_bf16_f32 v230, v188, v189\n\t" \
  "v_cvt_pk_bf16_f32 v231, v190, v191\n\t" \
  "v_exp_f32 v192, v192\n\t" \
  "v_exp_f32 v193, v193\n\t" \
  "s_waitcnt lgkmcnt(0)\n\t" \
  "v_mfma_f32_32x32x16_bf16 v[208:223], v[244:247], v[228:231], v[208:223]\n\t" \
  "v_exp_f32 v194, v194\n\t" \
  "v_exp_f32 v195, v195\n\t" \
  "v_exp_f32 v196, v196\n\t" \
  "v_exp_f32 v197, v197\n\t" \
  "v_mfma_f32_32x32x16_bf16 v[176:191], v[232:235], v[248:251], 0\n\t" \
  "v_cvt_pk_bf16_f32 v224, v192, v193\n\t" \
  "v_cvt_pk_bf16_f32 v225, v194, v195\n\t" \
  "v_exp_f32 v198, v198\n\t" \
  "v_exp_f32 v199, v199\n\t" \
  "v_exp_f32 v200, v200\n\t" \
  "v_exp_f32 v201, v201\n\t" \
  "v_mfma_f32_32x32x8_bf16 v[176:191], v[236:237], v[238:239], v[176:191]\n\t" \
  "v_cvt_pk_bf16_f32 v226, v196, v197\n\t" \
  "v_cvt_pk_bf16_f32 v227, v198, v199\n\t" \
  "v_exp_f32 v202, v202\n\t" \
  "v_exp_f32 v203, v203\n\t" \
  "v_exp_f32 v204, v204\n\t" \
  "v_exp_f32 v205, v205\n\t" \
  "v_mfma_f32_32x32x16_bf16 v[152:167], v[240:243], v[224:227], v[152:167]\n\t" \
  "ds_read_b128 v[240:243], v254 offset:10496\n\t" \
  "v_cvt_pk_bf16_f32 v228, v200, v201\n\t" \
  "v_cvt_pk_bf16_f32 v229, v202, v203\n\t" \
  "v_exp_f32 v206, v206\n\t" \
  "v_exp_f32 v207, v207\n\t" \
  "v_cvt_pk_bf16_f32 v230, v204, v205\n\t" \
  "v_cvt_pk_bf16_f32 v231, v206, v207\n\t" \
  "v_exp_f32 v176, v176\n\t" \
  "v_exp_f32 v177, v177\n\t" \
  "v_mfma_f32_32x32x16_bf16 v[152:167], v[244:247], v[228:231], v[152:167]\n\t" \
  "ds_read_b128 v[244:247], v254 offset:11168\n\t" \
  "v_exp_f32 v178, v178\n\t" \
  "v_exp_f32 v179, v179\n\t" \
  "v_exp_f32 v180, v180\n\t" \
  "v_exp_f32 v181, v181\n\t" \
  "v_mfma_f32_32x32x16_bf16 v[192:207], v[232:235], v[168:171], 0\n\t" \
  "ds_read_b128 v[232:235], v252 offset:15744\n\t" \
  "v_cvt_pk_bf16_f32 v224, v176, v177\n\t" \
  "v_cvt_pk_bf16_f32 v225, v178, v179\n\t" \
  "v_exp_f32 v182, v182\n\t" \
  "buffer_load_dwordx4 v[36:39], v255, %[srd], %[fwr] offen sc1\n\t" \
  "v_exp_f32 v183, v183\n\t" \
  "v_exp_f32 v184, v184\n\t" \
  "v_exp_f32 v185, v185\n\t" \
  "v_mfma_f32_32x32x8_bf16 v[192:207], v[236:237], v[172:173], v[192:207]\n\t" \
  "ds_read_b32 v236, v253 offset:15744\n\t" \
  "v_cvt_pk_bf16_f32 v226, v180, v181\n\t" \
  "v_cvt_pk_bf16_f32 v227, v182, v183\n\t" \
  "v_exp_f32 v186, v186\n\t" \
  "v_exp_f32 v187, v187\n\t" \
  "v_exp_f32 v188, v188\n\t" \
  "v_exp_f32 v189, v189\n\t" \
  "s_waitcnt lgkmcnt(3)\n\t" \
  "v_mfma_f32_32x32x16_bf16 v[208:223], v[240:243], v[224:227], v[208:223]\n\t" \
  "v_cvt_pk_bf16_f32 v228, v184, v185\n\t" \
  "v_cvt_pk_bf16_f32 v229, v186, v187\n\t" \
  "v_exp_f32 v190, v190\n\t" \
  "buffer_load_dwordx4 v[40:43], v255, %[srd], %[fwr] offen offset:1024 sc1\n\t" \
  "v_exp_f32 v191, v191\n\t" \
  "v_cvt_pk_bf16_f32 v230, v188, v189\n\t" \
  "v_cvt_pk_bf16_f32 v231, v190, v191\n\t" \
  "v_exp_f32 v192, v192\n\t" \
  "v_exp_f32 v193, v193\n\t" \
  "s_waitcnt lgkmcnt(0)\n\t" \
  "v_mfma_f32_32x32x16_bf16 v[208:223], v[244:247], v[228:231], v[208:223]\n\t" \
  "v_exp_f32 v194, v194\n\t" \
  "v_exp_f32 v195, v195\n\t" \
  "v_exp_f32 v196, v196\n\t" \
  "v_exp_f32 v197, v197\n\t" \
  "v_mfma_f32_32x32x16_bf16 v[176:191], v[232:235], v[248:251], 0\n\t" \
  "v_cvt_pk_bf16_f32 v224, v192, v193\n\t" \
  "v_cvt_pk_bf16_f32 v225, v194, v195\n\t" \
  "v_exp_f32 v198, v198\n\t" \
  "v_exp_f32 v199, v199\n\t" \
  "v_exp_f32 v200, v200\n\t" \
  "v_exp_f32 v201, v201\n\t" \
  "v_mfma_f32_32x32x8_bf16 v[176:191], v[236:237], v[238:239], v[176:191]\n\t" \
  "v_cvt_pk_bf16_f32 v226, v196, v197\n\t" \
  "v_cvt_pk_bf16_f32 v227, v198, v199\n\t" \
  "v_exp_f32 v202, v202\n\t" \
  "v_exp_f32 v203, v203\n\t" \
  "v_exp_f32 v204, v204\n\t" \
  "v_exp_f32 v205, v205\n\t" \
  "v_mfma_f32_32x32x16_bf16 v[152:167], v[240:243], v[224:227], v[152:167]\n\t" \
  "ds_read_b128 v[240:243], v254 offset:15744\n\t" \
  "v_cvt_pk_bf16_f32 v228, v200, v201\n\t" \
  "v_cvt_pk_bf16_f32 v229, v202, v203\n\t" \
  "v_exp_f32 v206, v206\n\t" \
  "v_exp_f32 v207, v207\n\t" \
  "v_cvt_pk_bf16_f32 v230, v204, v205\n\t" \
  "v_cvt_pk_bf16_f32 v231, v206, v207\n\t" \
  "v_exp_f32 v176, v176\n\t" \
  "v_exp_f32 v177, v177\n\t" \
  "v_mfma_f32_32x32x16_bf16 v[152:167], v[244:247], v[228:231], v[152:167]\n\t" \
  "ds_read_b128 v[244:247], v254 offset:16416\n\t" \
  "v_exp_f32 v178, v178\n\t" \
  "v_exp_f32 v179, v179\n\t" \
  "v_exp_f32 v180, v180\n\t" \
  "v_exp_f32 v181, v181\n\t" \
  "v_mfma_f32_32x32x16_bf16 v[192:207], v[232:235], v[168:171], 0\n\t" \
  "v_add_u32 v252, 0x5200, v252\n\t" \
  "ds_read_b128 v[232:235], v252 offset:0\n\t" \
  "v_cvt_pk_bf16_f32 v224, v176, v177\n\t" \
  "v_cvt_pk_bf16_f32 v225, v178, v179\n\t" \
  "v_exp_f32 v182, v182\n\t" \
  "v_exp_f32 v183, v183\n\t" \
  "v_exp_f32 v184, v184\n\t" \
  "v_exp_f32 v185, v185\n\t" \
  "v_mfma_f32_32x32x8_bf16 v[192:207], v[236:237], v[172:173], v[192:207]\n\t" \
  "v_add_u32 v253, 0x5200, v253\n\t" \
  "ds_read_b32 v236, v253 offset:0\n\t" \
  "v_cvt_pk_bf16_f32 v226, v180, v181\n\t" \
  "v_cvt_pk_bf16_f32 v227, v182, v183\n\t" \
  "v_exp_f32 v186, v186\n\t" \
  "v_exp_f32 v187, v187\n\t" \
  "v_exp_f32 v188, v188\n\t" \
  "v_exp_f32 v189, v189\n\t" \
  "s_waitcnt lgkmcnt(3)\n\t" \
  "v_mfma_f32_32x32x16_bf16 v[208:223], v[240:243], v[224:227], v[208:223]\n\t" \
  "v_cvt_pk_bf16_f32 v228, v184, v185\n\t" \
  "v_cvt_pk_bf16_f32 v229, v186, v187\n\t" \
  "v_exp_f32 v190, v190\n\t" \
  "v_exp_f32 v191, v191\n\t" \
  "v_cvt_pk_bf16_f32 v230, v188, v189\n\t" \
  "v_cvt_pk_bf16_f32 v231, v190, v191\n\t" \
  "v_exp_f32 v192, v192\n\t" \
  "v_exp_f32 v193, v193\n\t" \
  "s_waitcnt lgkmcnt(0)\n\t" \
  "v_mfma_f32_32x32x16_bf16 v[208:223], v[244:247], v[228:231], v[208:223]\n\t" \
  "v_exp_f32 v194, v194\n\t" \
  "v_exp_f32 v195, v195\n\t" \
  "v_exp_f32 v196, v196\n\t" \
  "v_exp_f32 v197, v197\n\t" \
  "v_mfma_f32_32x32x16_bf16 v[176:191], v[232:235], v[248:251], 0\n\t" \
  "v_cvt_pk_bf16_f32 v224, v192, v193\n\t" \
  "v_cvt_pk_bf16_f32 v225, v194, v195\n\t" \
  "v_exp_f32 v198, v198\n\t" \
  "v_exp_f32 v199, v199\n\t" \
  "v_exp_f32 v200, v200\n\t" \
  "v_exp_f32 v201, v201\n\t" \
  "v_mfma_f32_32x32x8_bf16 v[176:191], v[236:237], v[238:239], v[176:191]\n\t" \
  "v_cvt_pk_bf16_f32 v226, v196, v197\n\t" \
  "v_cvt_pk_bf16_f32 v227, v198, v199\n\t" \
  "v_exp_f32 v202, v202\n\t" \
  "v_exp_f32 v203, v203\n\t" \
  "v_exp_f32 v204, v204\n\t" \
  "v_exp_f32 v205, v205\n\t" \
  "v_mfma_f32_32x32x16_bf16 v[152:167], v[240:243], v[224:227], v[152:167]\n\t" \
  "v_add_u32 v254, 0x5200, v254\n\t" \
  "ds_read_b128 v[240:243], v254 offset:0\n\t" \
  "v_cvt_pk_bf16_f32 v228, v200, v201\n\t" \
  "v_cvt_pk_bf16_f32 v229, v202, v203\n\t" \
  "v_exp_f32 v206, v206\n\t" \
  "v_exp_f32 v207, v207\n\t" \
  "v_cvt_pk_bf16_f32 v230, v204, v205\n\t" \
  "v_cvt_pk_bf16_f32 v231, v206, v207\n\t" \
  "s_mov_b32 %[t0], 0x9c40\n\t" \
  "att_chk_%=:\n\t" \
  "s_waitcnt vmcnt(0)\n\t" \
  "v_cmp_eq_u64 vcc, %[tag], v[36:37]\n\t" \
  "v_cmp_eq_u64 %[t1], %[tag], v[38:39]\n\t" \
  "s_and_b64 vcc, vcc, %[t1]\n\t" \
  "v_cmp_eq_u64 %[t1], %[tag], v[40:41]\n\t" \
  "s_and_b64 vcc, vcc, %[t1]\n\t" \
  "v_cmp_eq_u64 %[t1], %[tag], v[42:43]\n\t" \
  "s_and_b64 vcc, vcc, %[t1]\n\t" \
  "s_cmp_eq_u64 vcc, -1\n\t" \
  "s_cbranch_scc1 att_ok_%=\n\t" \
  "s_sub_u32 %[t0], %[t0], 1\n\t" \
  "s_cmp_eq_u32 %[t0], 0\n\t" \
  "s_cbranch_scc1 att_ok_%=\n\t" \
  "s_sleep 8\n\t" \
  "buffer_load_dwordx4 v[36:39], v255, %[srd], %[fwr] offen sc1\n\t" \
  "buffer_load_dwordx4 v[40:43], v255, %[srd], %[fwr] offen offset:1024 sc1\n\t" \
  "s_branch att_chk_%=\n\t" \
  "att_ok_%=:\n\t" \
  "v_exp_f32 v176, v176\n\t" \
  "v_exp_f32 v177, v177\n\t" \
  "v_mfma_f32_32x32x16_bf16 v[152:167], v[244:247], v[228:231], v[152:167]\n\t" \
  "ds_read_b128 v[244:247], v254 offset:672\n\t" \
  "v_exp_f32 v178, v178\n\t" \
  "v_exp_f32 v179, v179\n\t" \
  "v_exp_f32 v180, v180\n\t" \
  "v_exp_f32 v181, v181\n\t" \
  "v_mfma_f32_32x32x16_bf16 v[192:207], v[232:235], v[168:171], 0\n\t" \
  "ds_read_b128 v[232:235], v252 offset:5248\n\t" \
  "v_cvt_pk_bf16_f32 v224, v176, v177\n\t" \
  "v_cvt_pk_bf16_f32 v225, v178, v179\n\t" \
  "v_exp_f32 v182, v182\n\t" \
  "s_add_u32 %[t0], %[w1s], 0\n\t" \
  "buffer_load_dwordx4 v[32:35], v255, %[srd], %[t0] offen offset:0 sc1\n\t" \
  "v_exp_f32 v183, v183\n\t" \
  "v_exp_f32 v184, v184\n\t" \
  "v_exp_f32 v185, v185\n\t" \
  "v_mfma_f32_32x32x8_bf16 v[192:207], v[236:237], v[172:173], v[192:207]\n\t" \
  "ds_read_b32 v236, v253 offset:5248\n\t" \
  "v_cvt_pk_bf16_f32 v226, v180, v181\n\t" \
  "v_cvt_pk_bf16_f32 v227, v182, v183\n\t" \
  "v_exp_f32 v186, v186\n\t" \
  "v_exp_f32 v187, v187\n\t" \
  "v_exp_f32 v188, v188\n\t" \
  "v_exp_f32 v189, v189\n\t" \
  "s_waitcnt lgkmcnt(3)\n\t" \
  "v_mfma_f32_32x32x16_bf16 v[208:223], v[240:243], v[224:227], v[208:223]\n\t" \
  "v_cvt_pk_bf16_f32 v228, v184, v185\n\t" \
  "v_cvt_pk_bf16_f32 v229, v186, v187\n\t" \
  "v_exp_f32 v190, v190\n\t" \
  "buffer_load_dwordx4 v[36:39], v255, %[srd], %[t0] offen offset:1024 sc1\n\t" \
  "v_exp_f32 v191, v191\n\t" \
  "v_cvt_pk_bf16_f32 v230, v188, v189\n\t" \
  "v_cvt_pk_bf16_f32 v231, v190, v191\n\t" \
  "v_exp_f32 v192, v192\n\t" \
  "v_exp_f32 v193, v193\n\t" \
  "s_waitcnt lgkmcnt(0)\n\t" \
  "v_mfma_f32_32x32x16_bf16 v[208:223], v[244:247], v[228:231], v[208:223]\n\t" \
  "v_exp_f32 v194, v194\n\t" \
  "v_exp_f32 v195, v195\n\t" \
  "v_exp_f32 v196, v196\n\t" \
  "v_exp_f32 v197, v197\n\t" \
  "v_mfma_f32_32x32x16_bf16 v[176:191], v[232:235], v[248:251], 0\n\t" \
  "v_cvt_pk_bf16_f32 v224, v192, v193\n\t" \
  "v_cvt_pk_bf16_f32 v225, v194, v195\n\t" \
  "v_exp_f32 v198, v198\n\t" \
  "buffer_load_dwordx4 v[40:43], v255, %[srd], %[t0] offen offset:2048 sc1\n\t" \
  "v_exp_f32 v199, v199\n\t" \
  "v_exp_f32 v200, v200\n\t" \
  "v_exp_f32 v201, v201\n\t" \
  "v_mfma_f32_32x32x8_bf16 v[176:191], v[236:237], v[238:239], v[176:191]\n\t" \
  "v_cvt_pk_bf16_f32 v226, v196, v197\n\t" \
  "v_cvt_pk_bf16_f32 v227, v198, v199\n\t" \
  "v_exp_f32 v202, v202\n\t" \
  "v_exp_f32 v203, v203\n\t" \
  "v_exp_f32 v204, v204\n\t" \
  "v_exp_f32 v205, v205\n\t" \
  "v_mfma_f32_32x32x16_bf16 v[152:167], v[240:243], v[224:227], v[152:167]\n\t" \
  "ds_read_b128 v[240:243], v254 offset:5248\n\t" \
  "v_cvt_pk_bf16_f32 v228, v200, v201\n\t" \
  "v_cvt_pk_bf16_f32 v229, v202, v203\n\t" \
  "v_exp_f32 v206, v206\n\t" \
  "buffer_load_dwordx4 v[44:47], v255, %[srd], %[t0] offen offset:3072 sc1\n\t" \
  "v_exp_f32 v207, v207\n\t" \
  "v_cvt_pk_bf16_f32 v230, v204, v205\n\t" \
  "v_cvt_pk_bf16_f32 v231, v206, v207\n\t" \
  "v_exp_f32 v176, v176\n\t" \
  "v_exp_f32 v177, v177\n\t" \
  "v_mfma_f32_32x32x16_bf16 v[152:167], v[244:247], v[228:231], v[152:167]\n\t" \
  "ds_read_b128 v[244:247], v254 offset:5920\n\t" \
  "v_exp_f32 v178, v178\n\t" \
  "v_exp_f32 v179, v179\n\t" \
  "v_exp_f32 v180, v180\n\t" \
  "v_exp_f32 v181, v181\n\t" \
  "v_mfma_f32_32x32x16_bf16 v[192:207], v[232:235], v[168:171], 0\n\t" \
  "ds_read_b128 v[232:235], v252 offset:10496\n\t" \
  "v_cvt_pk_bf16_f32 v224, v176, v177\n\t" \
  "v_cvt_pk_bf16_f32 v225, v178, v179\n\t" \
  "v_exp_f32 v182, v182\n\t" \
  "s_add_u32 %[t0], %[t0], 0x1000\n\t" \
  "buffer_load_dwordx4 v[48:51], v255, %[srd], %[t0] offen offset:0 sc1\n\t" \
  "v_exp_f32 v183, v183\n\t" \
  "v_exp_f32 v184, v184\n\t" \
  "v_exp_f32 v185, v185\n\t" \
  "v_mfma_f32_32x32x8_bf16 v[192:207], v[236:237], v[172:173], v[192:207]\n\t" \
  "ds_read_b32 v236, v253 offset:10496\n\t" \
  "v_cvt_pk_bf16_f32 v226, v180, v181\n\t" \
  "v_cvt_pk_bf16_f32 v227, v182, v183\n\t" \
  "v_exp_f32 v186, v186\n\t" \
  "v_exp_f32 v187, v187\n\t" \
  "v_exp_f32 v188, v188\n\t" \
  "v_exp_f32 v189, v189\n\t" \
  "s_waitcnt lgkmcnt(3)\n\t" \
  "v_mfma_f32_32x32x16_bf16 v[208:223], v[240:243], v[224:227], v[208:223]\n\t" \
  "v_cvt_pk_bf16_f32 v228, v184, v185\n\t" \
  "v_cvt_pk_bf16_f32 v229, v186, v187\n\t" \
  "v_exp_f32 v190, v190\n\t" \
  "buffer_load_dwordx4 v[52:55], v255, %[srd], %[t0] offen offset:1024 sc1\n\t" \
  "v_lshlrev_b32 v174, 3, v255\n\t" \
  "buffer_load_dword v175, v174, %[srd], %[w2t] offen sc1\n\t" \
  "v_exp_f32 v191, v191\n\t" \
  "v_cvt_pk_bf16_f32 v230, v188, v189\n\t" \
  "v_cvt_pk_bf16_f32 v231, v190, v191\n\t" \
  "v_exp_f32 v192, v192\n\t" \
  "v_exp_f32 v193, v193\n\t" \
  "s_waitcnt lgkmcnt(0)\n\t" \
  "v_mfma_f32_32x32x16_bf16 v[208:223], v[244:247], v[228:231], v[208:223]\n\t" \
  "v_exp_f32 v194, v194\n\t" \
  "v_exp_f32 v195, v195\n\t" \
  "v_exp_f32 v196, v196\n\t" \
  "v_exp_f32 v197, v197\n\t" \
  "v_mfma_f32_32x32x16_bf16 v[176:191], v[232:235], v[248:251], 0\n\t" \
  "v_cvt_pk_bf16_f32 v224, v192, v193\n\t" \
  "v_cvt_pk_bf16_f32 v225, v194, v195\n\t" \
  "v_exp_f32 v198, v198\n\t" \
  "buffer_load_dwordx4 v[56:59], v255, %[srd], %[t0] offen offset:2048 sc1\n\t" \
  "v_exp_f32 v199, v199\n\t" \
  "v_exp_f32 v200, v200\n\t" \
  "v_exp_f32 v201, v201\n\t" \
  "v_mfma_f32_32x32x8_bf16 v[176:191], v[236:237], v[238:239], v[176:191]\n\t" \
  "v_cvt_pk_bf16_f32 v226, v196, v197\n\t" \
  "v_cvt_pk_bf16_f32 v227, v198, v199\n\t" \
  "v_exp_f32 v202, v202\n\t" \
  "v_exp_f32 v203, v203\n\t" \
  "v_exp_f32 v204, v204\n\t" \
  "v_exp_f32 v205, v205\n\t" \
  "v_mfma_f32_32x32x16_bf16 v[152:167], v[240:243], v[224:227], v[152:167]\n\t" \
  "ds_read_b128 v[240:243], v254 offset:10496\n\t" \
  "v_cvt_pk_bf16_f32 v228, v200, v201\n\t" \
  "v_cvt_pk_bf16_f32 v229, v202, v203\n\t" \
  "v_exp_f32 v206, v206\n\t" \
  "buffer_load_dwordx4 v[60:63], v255, %[srd], %[t0] offen offset:3072 sc1\n\t" \
  "v_exp_f32 v207, v207\n\t" \
  "v_cvt_pk_bf16_f32 v230, v204, v205\n\t" \
  "v_cvt_pk_bf16_f32 v231, v206, v207\n\t" \
  "v_exp_f32 v176, v176\n\t" \
  "v_exp_f32 v177, v177\n\t" \
  "v_mfma_f32_32x32x16_bf16 v[152:167], v[244:247], v[228:231], v[152:167]\n\t" \
  "ds_read_b128 v[244:247], v254 offset:11168\n\t" \
  "v_exp_f32 v178, v178\n\t" \
  "v_exp_f32 v179, v179\n\t" \
  "v_exp_f32 v180, v180\n\t" \
  "v_exp_f32 v181, v181\n\t" \
  "v_mfma_f32_32x32x16_bf16 v[192:207], v[232:235], v[168:171], 0\n\t" \
  "ds_read_b128 v[232:235], v252 offset:15744\n\t" \
  "v_cvt_pk_bf16_f32 v224, v176, v177\n\t" \
  "v_cvt_pk_bf16_f32 v225, v178, v179\n\t" \
  "v_exp_f32 v182, v182\n\t" \
  "s_add_u32 %[t0], %[t0], 0x1000\n\t" \
  "buffer_load_dwordx4 v[64:67], v255, %[srd], %[t0] offen offset:0 sc1\n\t" \
  "v_exp_f32 v183, v183\n\t" \
  "v_exp_f32 v184, v184\n\t" \
  "v_exp_f32 v185, v185\n\t" \
  "v_mfma_f32_32x32x8_bf16 v[192:207], v[236:237], v[172:173], v[192:207]\n\t" \
  "ds_read_b32 v236, v253 offset:15744\n\t" \
  "v_cvt_pk_bf16_f32 v226, v180, v181\n\t" \
  "v_cvt_pk_bf16_f32 v227, v182, v183\n\t" \
  "v_exp_f32 v186, v186\n\t" \
  "v_exp_f32 v187, v187\n\t" \
  "v_exp_f32 v188, v188\n\t" \
  "v_exp_f32 v189, v189\n\t" \
  "s_waitcnt lgkmcnt(3)\n\t" \
  "v_mfma_f32_32x32x16_bf16 v[208:223], v[240:243], v[224:227], v[208:223]\n\t" \
  "v_cvt_pk_bf16_f32 v228, v184, v185\n\t" \
  "v_cvt_pk_bf16_f32 v229, v186, v187\n\t" \
  "v_exp_f32 v190, v190\n\t" \
  "buffer_load_dwordx4 v[68:71], v255, %[srd], %[t0] offen offset:1024 sc1\n\t" \
  "v_exp_f32 v191, v191\n\t" \
  "v_cvt_pk_bf16_f32 v230, v188, v189\n\t" \
  "v_cvt_pk_bf16_f32 v231, v190, v191\n\t" \
  "v_exp_f32 v192, v192\n\t" \
  "v_exp_f32 v193, v193\n\t" \
  "s_waitcnt lgkmcnt(0)\n\t" \
  "v_mfma_f32_32x32x16_bf16 v[208:223], v[244:247], v[228:231], v[208:223]\n\t" \
  "v_exp_f32 v194, v194\n\t" \
  "v_exp_f32 v195, v195\n\t" \
  "v_exp_f32 v196, v196\n\t" \
  "v_exp_f32 v197, v197\n\t" \
  "v_mfma_f32_32x32x16_bf16 v[176:191], v[232:235], v[248:251], 0\n\t" \
  "v_cvt_pk_bf16_f32 v224, v192, v193\n\t" \
  "v_cvt_pk_bf16_f32 v225, v194, v195\n\t" \
  "v_exp_f32 v198, v198\n\t" \
  "s_add_u32 %[t0], %[w1s], 20480\n\t" \
  "buffer_load_dwordx4 v[72:75], v255, %[srd], %[t0] offen offset:0 sc1\n\t" \
  "v_exp_f32 v199, v199\n\t" \
  "v_exp_f32 v200, v200\n\t" \
  "v_exp_f32 v201, v201\n\t" \
  "v_mfma_f32_32x32x8_bf16 v[176:191], v[236:237], v[238:239], v[176:191]\n\t" \
  "v_cvt_pk_bf16_f32 v226, v196, v197\n\t" \
  "v_cvt_pk_bf16_f32 v227, v198, v199\n\t" \
  "v_exp_f32 v202, v202\n\t" \
  "v_exp_f32 v203, v203\n\t" \
  "v_exp_f32 v204, v204\n\t" \
  "v_exp_f32 v205, v205\n\t" \
  "v_mfma_f32_32x32x16_bf16 v[152:167], v[240:243], v[224:227], v[152:167]\n\t" \
  "ds_read_b128 v[240:243], v254 offset:15744\n\t" \
  "v_cvt_pk_bf16_f32 v228, v200, v201\n\t" \
  "v_cvt_pk_bf16_f32 v229, v202, v203\n\t" \
  "v_exp_f32 v206, v206\n\t" \
  "buffer_load_dwordx4 v[76:79], v255, %[srd], %[t0] offen offset:1024 sc1\n\t" \
  "v_exp_f32 v207, v207\n\t" \
  "v_cvt_pk_bf16_f32 v230, v204, v205\n\t" \
  "v_cvt_pk_bf16_f32 v231, v206, v207\n\t" \
  "v_exp_f32 v176, v176\n\t" \
  "v_exp_f32 v177, v177\n\t" \
  "v_mfma_f32_32x32x16_bf16 v[152:167], v[244:247], v[228:231], v[152:167]\n\t" \
  "ds_read_b128 v[244:247], v254 offset:16416\n\t" \
  "v_exp_f32 v178, v178\n\t" \
  "v_exp_f32 v179, v179\n\t" \
  "v_exp_f32 v180, v180\n\t" \
  "v_exp_f32 v181, v181\n\t" \
  "v_mfma_f32_32x32x16_bf16 v[192:207], v[232:235], v[168:171], 0\n\t" \
  "v_add_u32 v252, 0x5200, v252\n\t" \
  "ds_read_b128 v[232:235], v252 offset:0\n\t" \
  "v_cvt_pk_bf16_f32 v224, v176, v177\n\t" \
  "v_cvt_pk_bf16_f32 v225, v178, v179\n\t" \
  "v_exp_f32 v182, v182\n\t" \
  "buffer_load_dwordx4 v[80:83], v255, %[srd], %[t0] offen offset:2048 sc1\n\t" \
  "v_exp_f32 v183, v183\n\t" \
  "v_exp_f32 v184, v184\n\t" \
  "v_exp_f32 v185, v185\n\t" \
  "v_mfma_f32_32x32x8_bf16 v[192:207], v[236:237], v[172:173], v[192:207]\n\t" \
  "v_add_u32 v253, 0x5200, v253\n\t" \
  "ds_read_b32 v236, v253 offset:0\n\t" \
  "v_cvt_pk_bf16_f32 v226, v180, v181\n\t" \
  "v_cvt_pk_bf16_f32 v227, v182, v183\n\t" \
  "v_exp_f32 v186, v186\n\t" \
  "v_exp_f32 v187, v187\n\t" \
  "v_exp_f32 v188, v188\n\t" \
  "v_exp_f32 v189, v189\n\t" \
  "s_waitcnt lgkmcnt(3)\n\t" \
  "v_mfma_f32_32x32x16_bf16 v[208:223], v[240:243], v[224:227], v[208:223]\n\t" \
  "v_cvt_pk_bf16_f32 v228, v184, v185\n\t" \
  "v_cvt_pk_bf16_f32 v229, v186, v187\n\t" \
  "v_exp_f32 v190, v190\n\t" \
  "buffer_load_dwordx4 v[84:87], v255, %[srd], %[t0] offen offset:3072 sc1\n\t" \
  "v_exp_f32 v191, v191\n\t" \
  "v_cvt_pk_bf16_f32 v230, v188, v189\n\t" \
  "v_cvt_pk_bf16_f32 v231, v190, v191\n\t" \
  "v_exp_f32 v192, v192\n\t" \
  "v_exp_f32 v193, v193\n\t" \
  "s_waitcnt lgkmcnt(0)\n\t" \
  "v_mfma_f32_32x32x16_bf16 v[208:223], v[244:247], v[228:231], v[208:223]\n\t" \
  "v_exp_f32 v194, v194\n\t" \
  "v_exp_f32 v195, v195\n\t" \
  "v_exp_f32 v196, v196\n\t" \
  "v_exp_f32 v197, v197\n\t" \
  "v_mfma_f32_32x32x16_bf16 v[176:191], v[232:235], v[248:251], 0\n\t" \
  "v_cvt_pk_bf16_f32 v224, v192, v193\n\t" \
  "v_cvt_pk_bf16_f32 v225, v194, v195\n\t" \
  "v_exp_f32 v198, v198\n\t" \
  "s_add_u32 %[t0], %[t0], 0x1000\n\t" \
  "buffer_load_dwordx4 v[88:91], v255, %[srd], %[t0] offen offset:0 sc1\n\t" \
  "v_exp_f32 v199, v199\n\t" \
  "v_exp_f32 v200, v200\n\t" \
  "v_exp_f32 v201, v201\n\t" \
  "v_mfma_f32_32x32x8_bf16 v[176:191], v[236:237], v[238:239], v[176:191]\n\t" \
  "v_cvt_pk_bf16_f32 v226, v196, v197\n\t" \
  "v_cvt_pk_bf16_f32 v227, v198, v199\n\t" \
  "v_exp_f32 v202, v202\n\t" \
  "v_exp_f32 v203, v203\n\t" \
  "v_exp_f32 v204, v204\n\t" \
  "v_exp_f32 v205, v205\n\t" \
  "v_mfma_f32_32x32x16_bf16 v[152:167], v[240:243], v[224:227], v[152:167]\n\t" \
  "v_add_u32 v254, 0x5200, v254\n\t" \
  "ds_read_b128 v[240:243], v254 offset:0\n\t" \
  "v_cvt_pk_bf16_f32 v228, v200, v201\n\t" \
  "v_cvt_pk_bf16_f32 v229, v202, v203\n\t" \
  "v_exp_f32 v206, v206\n\t" \
  "buffer_load_dwordx4 v[92:95], v255, %[srd], %[t0] offen offset:1024 sc1\n\t" \
  "v_exp_f32 v207, v207\n\t" \
  "v_cvt_pk_bf16_f32 v230, v204, v205\n\t" \
  "v_cvt_pk_bf16_f32 v231, v206, v207\n\t" \
  "v_exp_f32 v176, v176\n\t" \
  "v_exp_f32 v177, v177\n\t" \
  "v_mfma_f32_32x32x16_bf16 v[152:167], v[244:247], v[228:231], v[152:167]\n\t" \
  "ds_read_b128 v[244:247], v254 offset:672\n\t" \
  "v_exp_f32 v178, v178\n\t" \
  "v_exp_f32 v179, v179\n\t" \
  "v_exp_f32 v180, v180\n\t" \
  "v_exp_f32 v181, v181\n\t" \
  "v_mfma_f32_32x32x16_bf16 v[192:207], v[232:235], v[168:171], 0\n\t" \
  "ds_read_b128 v[232:235], v252 offset:5248\n\t" \
  "v_cvt_pk_bf16_f32 v224, v176, v177\n\t" \
  "v_cvt_pk_bf16_f32 v225, v178, v179\n\t" \
  "v_exp_f32 v182, v182\n\t" \
  "buffer_load_dwordx4 v[96:99], v255, %[srd], %[t0] offen offset:2048 sc1\n\t" \
  "v_exp_f32 v183, v183\n\t" \
  "v_exp_f32 v184, v184\n\t" \
  "v_exp_f32 v185, v185\n\t" \
  "v_mfma_f32_32x32x8_bf16 v[192:207], v[236:237], v[172:173], v[192:207]\n\t" \
  "ds_read_b32 v236, v253 offset:5248\n\t" \
  "v_cvt_pk_bf16_f32 v226, v180, v181\n\t" \
  "v_cvt_pk_bf16_f32 v227, v182, v183\n\t" \
  "v_exp_f32 v186, v186\n\t" \
  "v_exp_f32 v187, v187\n\t" \
  "v_exp_f32 v188, v188\n\t" \
  "v_exp_f32 v189, v189\n\t" \
  "s_waitcnt lgkmcnt(3)\n\t" \
  "v_mfma_f32_32x32x16_bf16 v[208:223], v[240:243], v[224:227], v[208:223]\n\t" \
  "v_cvt_pk_bf16_f32 v228, v184, v185\n\t" \
  "v_cvt_pk_bf16_f32 v229, v186, v187\n\t" \
  "v_exp_f32 v190, v190\n\t" \
  "buffer_load_dwordx4 v[100:103], v255, %[srd], %[t0] offen offset:3072 sc1\n\t" \
  "v_exp_f32 v191, v191\n\t" \
  "v_cvt_pk_bf16_f32 v230, v188, v189\n\t" \
  "v_cvt_pk_bf16_f32 v231, v190, v191\n\t" \
  "v_exp_f32 v192, v192\n\t" \
  "v_exp_f32 v193, v193\n\t" \
  "s_waitcnt lgkmcnt(0)\n\t" \
  "v_mfma_f32_32x32x16_bf16 v[208:223], v[244:247], v[228:231], v[208:223]\n\t" \
  "v_exp_f32 v194, v194\n\t" \
  "v_exp_f32 v195, v195\n\t" \
  "v_exp_f32 v196, v196\n\t" \
  "v_exp_f32 v197, v197\n\t" \
  "v_mfma_f32_32x32x16_bf16 v[176:191], v[232:235], v[248:251], 0\n\t" \
  "v_cvt_pk_bf16_f32 v224, v192, v193\n\t" \
  "v_cvt_pk_bf16_f32 v225, v194, v195\n\t" \
  "v_exp_f32 v198, v198\n\t" \
  "s_add_u32 %[t0], %[t0], 0x1000\n\t" \
  "buffer_load_dwordx4 v[104:107], v255, %[srd], %[t0] offen offset:0 sc1\n\t" \
  "v_exp_f32 v199, v199\n\t" \
  "v_exp_f32 v200, v200\n\t" \
  "v_exp_f32 v201, v201\n\t" \
  "v_mfma_f32_32x32x8_bf16 v[176:191], v[236:237], v[238:239], v[176:191]\n\t" \
  "v_cvt_pk_bf16_f32 v226, v196, v197\n\t" \
  "v_cvt_pk_bf16_f32 v227, v198, v199\n\t" \
  "v_exp_f32 v202, v202\n\t" \
  "v_exp_f32 v203, v203\n\t" \
  "v_exp_f32 v204, v204\n\t" \
  "v_exp_f32 v205, v205\n\t" \
  "v_mfma_f32_32x32x16_bf16 v[152:167], v[240:243], v[224:227], v[152:167]\n\t" \
  "ds_read_b128 v[240:243], v254 offset:5248\n\t" \
  "v_cvt_pk_bf16_f32 v228, v200, v201\n\t" \
  "v_cvt_pk_bf16_f32 v229, v202, v203\n\t" \
  "v_exp_f32 v206, v206\n\t" \
  "buffer_load_dwordx4 v[108:111], v255, %[srd], %[t0] offen offset:1024 sc1\n\t" \
  "v_exp_f32 v207, v207\n\t" \
  "v_cvt_pk_bf16_f32 v230, v204, v205\n\t" \
  "v_cvt_pk_bf16_f32 v231, v206, v207\n\t" \
  "v_exp_f32 v176, v176\n\t" \
  "v_exp_f32 v177, v177\n\t" \
  "v_mfma_f32_32x32x16_bf16 v[152:167], v[244:247], v[228:231], v[152:167]\n\t" \
  "ds_read_b128 v[244:247], v254 offset:5920\n\t" \
  "v_exp_f32 v178, v178\n\t" \
  "v_exp_f32 v179, v179\n\t" \
  "v_exp_f32 v180, v180\n\t" \
  "v_exp_f32 v181, v181\n\t" \
  "v_mfma_f32_32x32x16_bf16 v[192:207], v[232:235], v[168:171], 0\n\t" \
  "ds_read_b128 v[232:235], v252 offset:10496\n\t" \
  "v_cvt_pk_bf16_f32 v224, v176, v177\n\t" \
  "v_cvt_pk_bf16_f32 v225, v178, v179\n\t" \
  "v_exp_f32 v182, v182\n\t" \
  "s_add_u32 %[t0], %[w1s], 40960\n\t" \
  "buffer_load_dwordx4 v[112:115], v255, %[srd], %[t0] offen offset:0 sc1\n\t" \
  "v_exp_f32 v183, v183\n\t" \
  "v_exp_f32 v184, v184\n\t" \
  "v_exp_f32 v185, v185\n\t" \
  "v_mfma_f32_32x32x8_bf16 v[192:207], v[236:237], v[172:173], v[192:207]\n\t" \
  "ds_read_b32 v236, v253 offset:10496\n\t" \
  "v_cvt_pk_bf16_f32 v226, v180, v181\n\t" \
  "v_cvt_pk_bf16_f32 v227, v182, v183\n\t" \
  "v_exp_f32 v186, v186\n\t" \
  "v_exp_f32 v187, v187\n\t" \
  "v_exp_f32 v188, v188\n\t" \
  "v_exp_f32 v189, v189\n\t" \
  "s_waitcnt lgkmcnt(3)\n\t" \
  "v_mfma_f32_32x32x16_bf16 v[208:223], v[240:243], v[224:227], v[208:223]\n\t" \
  "v_cvt_pk_bf16_f32 v228, v184, v185\n\t" \
  "v_cvt_pk_bf16_f32 v229, v186, v187\n\t" \
  "v_exp_f32 v190, v190\n\t" \
  "buffer_load_dwordx4 v[116:119], v255, %[srd], %[t0] offen offset:1024 sc1\n\t" \
  "v_exp_f32 v191, v191\n\t" \
  "v_cvt_pk_bf16_f32 v230, v188, v189\n\t" \
  "v_cvt_pk_bf16_f32 v231, v190, v191\n\t" \
  "v_exp_f32 v192, v192\n\t" \
  "v_exp_f32 v193, v193\n\t" \
  "s_waitcnt lgkmcnt(0)\n\t" \
  "v_mfma_f32_32x32x16_bf16 v[208:223], v[244:247], v[228:231], v[208:223]\n\t" \
  "v_exp_f32 v194, v194\n\t" \
  "v_exp_f32 v195, v195\n\t" \
  "v_exp_f32 v196, v196\n\t" \
  "v_exp_f32 v197, v197\n\t" \
  "v_mfma_f32_32x32x16_bf16 v[176:191], v[232:235], v[248:251], 0\n\t" \
  "v_cvt_pk_bf16_f32 v224, v192, v193\n\t" \
  "v_cvt_pk_bf16_f32 v225, v194, v195\n\t" \
  "v_exp_f32 v198, v198\n\t" \
  "buffer_load_dwordx4 v[120:123], v255, %[srd], %[t0] offen offset:2048 sc1\n\t" \
  "v_exp_f32 v199, v199\n\t" \
  "v_exp_f32 v200, v200\n\t" \
  "v_exp_f32 v201, v201\n\t" \
  "v_mfma_f32_32x32x8_bf16 v[176:191], v[236:237], v[238:239], v[176:191]\n\t" \
  "v_cvt_pk_bf16_f32 v226, v196, v197\n\t" \
  "v_cvt_pk_bf16_f32 v227, v198, v199\n\t" \
  "v_exp_f32 v202, v202\n\t" \
  "v_exp_f32 v203, v203\n\t" \
  "v_exp_f32 v204, v204\n\t" \
  "v_exp_f32 v205, v205\n\t" \
  "v_mfma_f32_32x32x16_bf16 v[152:167], v[240:243], v[224:227], v[152:167]\n\t" \
  "ds_read_b128 v[240:243], v254 offset:10496\n\t" \
  "v_cvt_pk_bf16_f32 v228, v200, v201\n\t" \
  "v_cvt_pk_bf16_f32 v229, v202, v203\n\t" \
  "v_exp_f32 v206, v206\n\t" \
  "buffer_load_dwordx4 v[124:127], v255, %[srd], %[t0] offen offset:3072 sc1\n\t" \
  "v_exp_f32 v207, v207\n\t" \
  "v_cvt_pk_bf16_f32 v230, v204, v205\n\t" \
  "v_cvt_pk_bf16_f32 v231, v206, v207\n\t" \
  "v_exp_f32 v176, v176\n\t" \
  "v_exp_f32 v177, v177\n\t" \
  "v_mfma_f32_32x32x16_bf16 v[152:167], v[244:247], v[228:231], v[152:167]\n\t" \
  "ds_read_b128 v[244:247], v254 offset:11168\n\t" \
  "v_exp_f32 v178, v178\n\t" \
  "v_exp_f32 v179, v179\n\t" \
  "v_exp_f32 v180, v180\n\t" \
  "v_exp_f32 v181, v181\n\t" \
  "v_mfma_f32_32x32x16_bf16 v[192:207], v[232:235], v[168:171], 0\n\t" \
  "ds_read_b128 v[232:235], v252 offset:15744\n\t" \
  "v_cvt_pk_bf16_f32 v224, v176, v177\n\t" \
  "v_cvt_pk_bf16_f32 v225, v178, v179\n\t" \
  "v_exp_f32 v182, v182\n\t" \
  "s_add_u32 %[t0], %[t0], 0x1000\n\t" \
  "buffer_load_dwordx4 v[128:131], v255, %[srd], %[t0] offen offset:0 sc1\n\t" \
  "v_exp_f32 v183, v183\n\t" \
  "v_exp_f32 v184, v184\n\t" \
  "v_exp_f32 v185, v185\n\t" \
  "v_mfma_f32_32x32x8_bf16 v[192:207], v[236:237], v[172:173], v[192:207]\n\t" \
  "ds_read_b32 v236, v253 offset:15744\n\t" \
  "v_cvt_pk_bf16_f32 v226, v180, v181\n\t" \
  "v_cvt_pk_bf16_f32 v227, v182, v183\n\t" \
  "v_exp_f32 v186, v186\n\t" \
  "v_exp_f32 v187, v187\n\t" \
  "v_exp_f32 v188, v188\n\t" \
  "v_exp_f32 v189, v189\n\t" \
  "s_waitcnt lgkmcnt(3)\n\t" \
  "v_mfma_f32_32x32x16_bf16 v[208:223], v[240:243], v[224:227], v[208:223]\n\t" \
  "v_cvt_pk_bf16_f32 v228, v184, v185\n\t" \
  "v_cvt_pk_bf16_f32 v229, v186, v187\n\t" \
  "v_exp_f32 v190, v190\n\t" \
  "buffer_load_dwordx4 v[132:135], v255, %[srd], %[t0] offen offset:1024 sc1\n\t" \
  "v_exp_f32 v191, v191\n\t" \
  "v_cvt_pk_bf16_f32 v230, v188, v189\n\t" \
  "v_cvt_pk_bf16_f32 v231, v190, v191\n\t" \
  "v_exp_f32 v192, v192\n\t" \
  "v_exp_f32 v193, v193\n\t" \
  "s_waitcnt lgkmcnt(0)\n\t" \
  "v_mfma_f32_32x32x16_bf16 v[208:223], v[244:247], v[228:231], v[208:223]\n\t" \
  "v_exp_f32 v194, v194\n\t" \
  "v_exp_f32 v195, v195\n\t" \
  "v_exp_f32 v196, v196\n\t" \
  "v_exp_f32 v197, v197\n\t" \
  "v_mfma_f32_32x32x16_bf16 v[176:191], v[232:235], v[248:251], 0\n\t" \
  "v_cvt_pk_bf16_f32 v224, v192, v193\n\t" \
  "v_cvt_pk_bf16_f32 v225, v194, v195\n\t" \
  "v_exp_f32 v198, v198\n\t" \
  "buffer_load_dwordx4 v[136:139], v255, %[srd], %[t0] offen offset:2048 sc1\n\t" \
  "v_exp_f32 v199, v199\n\t" \
  "v_exp_f32 v200, v200\n\t" \
  "v_exp_f32 v201, v201\n\t" \
  "v_mfma_f32_32x32x8_bf16 v[176:191], v[236:237], v[238:239], v[176:191]\n\t" \
  "v_cvt_pk_bf16_f32 v226, v196, v197\n\t" \
  "v_cvt_pk_bf16_f32 v227, v198, v199\n\t" \
  "v_exp_f32 v202, v202\n\t" \
  "v_exp_f32 v203, v203\n\t" \
  "v_exp_f32 v204, v204\n\t" \
  "v_exp_f32 v205, v205\n\t" \
  "v_mfma_f32_32x32x16_bf16 v[152:167], v[240:243], v[224:227], v[152:167]\n\t" \
  "ds_read_b128 v[240:243], v254 offset:15744\n\t" \
  "v_cvt_pk_bf16_f32 v228, v200, v201\n\t" \
  "v_cvt_pk_bf16_f32 v229, v202, v203\n\t" \
  "v_exp_f32 v206, v206\n\t" \
  "buffer_load_dwordx4 v[140:143], v255, %[srd], %[t0] offen offset:3072 sc1\n\t" \
  "v_exp_f32 v207, v207\n\t" \
  "v_cvt_pk_bf16_f32 v230, v204, v205\n\t" \
  "v_cvt_pk_bf16_f32 v231, v206, v207\n\t" \
  "v_exp_f32 v176, v176\n\t" \
  "v_exp_f32 v177, v177\n\t" \
  "v_mfma_f32_32x32x16_bf16 v[152:167], v[244:247], v[228:231], v[152:167]\n\t" \
  "ds_read_b128 v[244:247], v254 offset:16416\n\t" \
  "v_exp_f32 v178, v178\n\t" \
  "v_exp_f32 v179, v179\n\t" \
  "v_exp_f32 v180, v180\n\t" \
  "v_exp_f32 v181, v181\n\t" \
  "v_mfma_f32_32x32x16_bf16 v[192:207], v[232:235], v[168:171], 0\n\t" \
  "v_add_u32 v252, 0x5200, v252\n\t" \
  "ds_read_b128 v[232:235], v252 offset:0\n\t" \
  "v_cvt_pk_bf16_f32 v224, v176, v177\n\t" \
  "v_cvt_pk_bf16_f32 v225, v178, v179\n\t" \
  "v_exp_f32 v182, v182\n\t" \
  "s_add_u32 %[t0], %[t0], 0x1000\n\t" \
  "buffer_load_dwordx4 v[144:147], v255, %[srd], %[t0] offen offset:0 sc1\n\t" \
  "v_exp_f32 v183, v183\n\t" \
  "v_exp_f32 v184, v184\n\t" \
  "v_exp_f32 v185, v185\n\t" \
  "v_mfma_f32_32x32x8_bf16 v[192:207], v[236:237], v[172:173], v[192:207]\n\t" \
  "v_add_u32 v253, 0x5200, v253\n\t" \
  "ds_read_b32 v236, v253 offset:0\n\t" \
  "v_cvt_pk_bf16_f32 v226, v180, v181\n\t" \
  "v_cvt_pk_bf16_f32 v227, v182, v183\n\t" \
  "v_exp_f32 v186, v186\n\t" \
  "v_exp_f32 v187, v187\n\t" \
  "v_exp_f32 v188, v188\n\t" \
  "v_exp_f32 v189, v189\n\t" \
  "s_waitcnt lgkmcnt(3)\n\t" \
  "v_mfma_f32_32x32x16_bf16 v[208:223], v[240:243], v[224:227], v[208:223]\n\t" \
  "v_cvt_pk_bf16_f32 v228, v184, v185\n\t" \
  "v_cvt_pk_bf16_f32 v229, v186, v187\n\t" \
  "v_exp_f32 v190, v190\n\t" \
  "buffer_load_dwordx4 v[148:151], v255, %[srd], %[t0] offen offset:1024 sc1\n\t" \
  "v_exp_f32 v191, v191\n\t" \
  "v_cvt_pk_bf16_f32 v230, v188, v189\n\t" \
  "v_cvt_pk_bf16_f32 v231, v190, v191\n\t" \
  "v_exp_f32 v192, v192\n\t" \
  "v_exp_f32 v193, v193\n\t" \
  "s_waitcnt lgkmcnt(0)\n\t" \
  "s_barrier\n\t" \
  "v_mfma_f32_32x32x16_bf16 v[208:223], v[244:247], v[228:231], v[208:223]\n\t" \
  "v_exp_f32 v194, v194\n\t" \
  "v_exp_f32 v195, v195\n\t" \
  "v_exp_f32 v196, v196\n\t" \
  "v_exp_f32 v197, v197\n\t" \
  "v_mfma_f32_32x32x16_bf16 v[176:191], v[232:235], v[248:251], 0\n\t" \
  "v_cvt_pk_bf16_f32 v224, v192, v193\n\t" \
  "v_cvt_pk_bf16_f32 v225, v194, v195\n\t" \
  "v_exp_f32 v198, v198\n\t" \
  "s_mov_b32 m0, %[wom]\n\t" \
  "s_mov_b32 %[t0], %[wos]\n\t" \
  "buffer_load_dwordx4 v255, %[srd], %[t0] offen offset:0 sc1 lds\n\t" \
  "v_exp_f32 v199, v199\n\t" \
  "v_exp_f32 v200, v200\n\t" \
  "v_exp_f32 v201, v201\n\t" \
  "v_mfma_f32_32x32x8_bf16 v[176:191], v[236:237], v[238:239], v[176:191]\n\t" \
  "v_cvt_pk_bf16_f32 v226, v196, v197\n\t" \
  "v_cvt_pk_bf16_f32 v227, v198, v199\n\t" \
  "v_exp_f32 v202, v202\n\t" \
  "v_exp_f32 v203, v203\n\t" \
  "v_exp_f32 v204, v204\n\t" \
  "v_exp_f32 v205, v205\n\t" \
  "v_mfma_f32_32x32x16_bf16 v[152:167], v[240:243], v[224:227], v[152:167]\n\t" \
  "v_add_u32 v254, 0x5200, v254\n\t" \
  "ds_read_b128 v[240:243], v254 offset:0\n\t" \
  "v_cvt_pk_bf16_f32 v228, v200, v201\n\t" \
  "v_cvt_pk_bf16_f32 v229, v202, v203\n\t" \
  "v_exp_f32 v206, v206\n\t" \
  "buffer_load_dwordx4 v255, %[srd], %[t0] offen offset:1024 sc1 lds\n\t" \
  "v_exp_f32 v207, v207\n\t" \
  "v_cvt_pk_bf16_f32 v230, v204, v205\n\t" \
  "v_cvt_pk_bf16_f32 v231, v206, v207\n\t" \
  "v_exp_f32 v176, v176\n\t" \
  "v_exp_f32 v177, v177\n\t" \
  "v_mfma_f32_32x32x16_bf16 v[152:167], v[244:247], v[228:231], v[152:167]\n\t" \
  "ds_read_b128 v[244:247], v254 offset:672\n\t" \
  "v_exp_f32 v178, v178\n\t" \
  "v_exp_f32 v179, v179\n\t" \
  "v_exp_f32 v180, v180\n\t" \
  "v_exp_f32 v181, v181\n\t" \
  "v_mfma_f32_32x32x16_bf16 v[192:207], v[232:235], v[168:171], 0\n\t" \
  "ds_read_b128 v[232:235], v252 offset:5248\n\t" \
  "v_cvt_pk_bf16_f32 v224, v176, v177\n\t" \
  "v_cvt_pk_bf16_f32 v225, v178, v179\n\t" \
  "v_exp_f32 v182, v182\n\t" \
  "buffer_load_dwordx4 v255, %[srd], %[t0] offen offset:2048 sc1 lds\n\t" \
  "v_exp_f32 v183, v183\n\t" \
  "v_exp_f32 v184, v184\n\t" \
  "v_exp_f32 v185, v185\n\t" \
  "v_mfma_f32_32x32x8_bf16 v[192:207], v[236:237], v[172:173], v[192:207]\n\t" \
  "ds_read_b32 v236, v253 offset:5248\n\t" \
  "v_cvt_pk_bf16_f32 v226, v180, v181\n\t" \
  "v_cvt_pk_bf16_f32 v227, v182, v183\n\t" \
  "v_exp_f32 v186, v186\n\t" \
  "v_exp_f32 v187, v187\n\t" \
  "v_exp_f32 v188, v188\n\t" \
  "v_exp_f32 v189, v189\n\t" \
  "s_waitcnt lgkmcnt(3)\n\t" \
  "v_mfma_f32_32x32x16_bf16 v[208:223], v[240:243], v[224:227], v[208:223]\n\t" \
  "v_cvt_pk_bf16_f32 v228, v184, v185\n\t" \
  "v_cvt_pk_bf16_f32 v229, v186, v187\n\t" \
  "v_exp_f32 v190, v190\n\t" \
  "buffer_load_dwordx4 v255, %[srd], %[t0] offen offset:3072 sc1 lds\n\t" \
  "v_exp_f32 v191, v191\n\t" \
  "v_cvt_pk_bf16_f32 v230, v188, v189\n\t" \
  "v_cvt_pk_bf16_f32 v231, v190, v191\n\t" \
  "v_exp_f32 v192, v192\n\t" \
  "v_exp_f32 v193, v193\n\t" \
  "s_waitcnt lgkmcnt(0)\n\t" \
  "v_mfma_f32_32x32x16_bf16 v[208:223], v[244:247], v[228:231], v[208:223]\n\t" \
  "v_exp_f32 v194, v194\n\t" \
  "v_exp_f32 v195, v195\n\t" \
  "v_exp_f32 v196, v196\n\t" \
  "v_exp_f32 v197, v197\n\t" \
  "v_mfma_f32_32x32x16_bf16 v[176:191], v[232:235], v[248:251], 0\n\t" \
  "v_cvt_pk_bf16_f32 v224, v192, v193\n\t" \
  "v_cvt_pk_bf16_f32 v225, v194, v195\n\t" \
  "v_exp_f32 v198, v198\n\t" \
  "s_add_u32 m0, m0, 0x1000\n\t" \
  "s_add_u32 %[t0], %[t0], 0x1000\n\t" \
  "buffer_load_dwordx4 v255, %[srd], %[t0] offen offset:0 sc1 lds\n\t" \
  "v_exp_f32 v199, v199\n\t" \
  "v_exp_f32 v200, v200\n\t" \
  "v_exp_f32 v201, v201\n\t" \
  "v_mfma_f32_32x32x8_bf16 v[176:191], v[236:237], v[238:239], v[176:191]\n\t" \
  "v_cvt_pk_bf16_f32 v226, v196, v197\n\t" \
  "v_cvt_pk_bf16_f32 v227, v198, v199\n\t" \
  "v_exp_f32 v202, v202\n\t" \
  "v_exp_f32 v203, v203\n\t" \
  "v_exp_f32 v204, v204\n\t" \
  "v_exp_f32 v205, v205\n\t" \
  "v_mfma_f32_32x32x16_bf16 v[152:167], v[240:243], v[224:227], v[152:167]\n\t" \
  "ds_read_b128 v[240:243], v254 offset:5248\n\t" \
  "v_cvt_pk_bf16_f32 v228, v200, v201\n\t" \
  "v_cvt_pk_bf16_f32 v229, v202, v203\n\t" \
  "v_exp_f32 v206, v206\n\t" \
  "buffer_load_dwordx4 v255, %[srd], %[t0] offen offset:1024 sc1 lds\n\t" \
  "v_exp_f32 v207, v207\n\t" \
  "v_cvt_pk_bf16_f32 v230, v204, v205\n\t" \
  "v_cvt_pk_bf16_f32 v231, v206, v207\n\t" \
  "v_exp_f32 v176, v176\n\t" \
  "v_exp_f32 v177, v177\n\t" \
  "v_mfma_f32_32x32x16_bf16 v[152:167], v[244:247], v[228:231], v[152:167]\n\t" \
  "ds_read_b128 v[244:247], v254 offset:5920\n\t" \
  "v_exp_f32 v178, v178\n\t" \
  "v_exp_f32 v179, v179\n\t" \
  "v_exp_f32 v180, v180\n\t" \
  "v_exp_f32 v181, v181\n\t" \
  "v_mfma_f32_32x32x16_bf16 v[192:207], v[232:235], v[168:171], 0\n\t" \
  "ds_read_b128 v[232:235], v252 offset:10496\n\t" \
  "v_cvt_pk_bf16_f32 v224, v176, v177\n\t" \
  "v_cvt_pk_bf16_f32 v225, v178, v179\n\t" \
  "v_exp_f32 v182, v182\n\t" \
  "buffer_load_dwordx4 v255, %[srd], %[t0] offen offset:2048 sc1 lds\n\t" \
  "v_exp_f32 v183, v183\n\t" \
  "v_exp_f32 v184, v184\n\t" \
  "v_exp_f32 v185, v185\n\t" \
  "v_mfma_f32_32x32x8_bf16 v[192:207], v[236:237], v[172:173], v[192:207]\n\t" \
  "ds_read_b32 v236, v253 offset:10496\n\t" \
  "v_cvt_pk_bf16_f32 v226, v180, v181\n\t" \
  "v_cvt_pk_bf16_f32 v227, v182, v183\n\t" \
  "v_exp_f32 v186, v186\n\t" \
  "v_exp_f32 v187, v187\n\t" \
  "v_exp_f32 v188, v188\n\t" \
  "v_exp_f32 v189, v189\n\t" \
  "s_waitcnt lgkmcnt(3)\n\t" \
  "v_mfma_f32_32x32x16_bf16 v[208:223], v[240:243], v[224:227], v[208:223]\n\t" \
  "v_cvt_pk_bf16_f32 v228, v184, v185\n\t" \
  "v_cvt_pk_bf16_f32 v229, v186, v187\n\t" \
  "v_exp_f32 v190, v190\n\t" \
  "buffer_load_dwordx4 v255, %[srd], %[t0] offen offset:3072 sc1 lds\n\t" \
  "v_exp_f32 v191, v191\n\t" \
  "v_cvt_pk_bf16_f32 v230, v188, v189\n\t" \
  "v_cvt_pk_bf16_f32 v231, v190, v191\n\t" \
  "v_exp_f32 v192, v192\n\t" \
  "v_exp_f32 v193, v193\n\t" \
  "s_waitcnt lgkmcnt(0)\n\t" \
  "v_mfma_f32_32x32x16_bf16 v[208:223], v[244:247], v[228:231], v[208:223]\n\t" \
  "v_exp_f32 v194, v194\n\t" \
  "v_exp_f32 v195, v195\n\t" \
  "v_exp_f32 v196, v196\n\t" \
  "v_exp_f32 v197, v197\n\t" \
  "v_mfma_f32_32x32x16_bf16 v[176:191], v[232:235], v[248:251], 0\n\t" \
  "v_cvt_pk_bf16_f32 v224, v192, v193\n\t" \
  "v_cvt_pk_bf16_f32 v225, v194, v195\n\t" \
  "v_exp_f32 v198, v198\n\t" \
  "s_add_u32 m0, m0, 0x1000\n\t" \
  "s_add_u32 %[t0], %[t0], 0x1000\n\t" \
  "buffer_load_dwordx4 v255, %[srd], %[t0] offen offset:0 sc1 lds\n\t" \
  "v_exp_f32 v199, v199\n\t" \
  "v_exp_f32 v200, v200\n\t" \
  "v_exp_f32 v201, v201\n\t" \
  "v_mfma_f32_32x32x8_bf16 v[176:191], v[236:237], v[238:239], v[176:191]\n\t" \
  "v_cvt_pk_bf16_f32 v226, v196, v197\n\t" \
  "v_cvt_pk_bf16_f32 v227, v198, v199\n\t" \
  "v_exp_f32 v202, v202\n\t" \
  "v_exp_f32 v203, v203\n\t" \
  "v_exp_f32 v204, v204\n\t" \
  "v_exp_f32 v205, v205\n\t" \
  "v_mfma_f32_32x32x16_bf16 v[152:167], v[240:243], v[224:227], v[152:167]\n\t" \
  "ds_read_b128 v[240:243], v254 offset:10496\n\t" \
  "v_cvt_pk_bf16_f32 v228, v200, v201\n\t" \
  "v_cvt_pk_bf16_f32 v229, v202, v203\n\t" \
  "v_exp_f32 v206, v206\n\t" \
  "buffer_load_dwordx4 v255, %[srd], %[t0] offen offset:1024 sc1 lds\n\t" \
  "v_exp_f32 v207, v207\n\t" \
  "v_cvt_pk_bf16_f32 v230, v204, v205\n\t" \
  "v_cvt_pk_bf16_f32 v231, v206, v207\n\t" \
  "v_exp_f32 v176, v176\n\t" \
  "v_exp_f32 v177, v177\n\t" \
  "v_mfma_f32_32x32x16_bf16 v[152:167], v[244:247], v[228:231], v[152:167]\n\t" \
  "ds_read_b128 v[244:247], v254 offset:11168\n\t" \
  "v_exp_f32 v178, v178\n\t" \
  "v_exp_f32 v179, v179\n\t" \
  "v_exp_f32 v180, v180\n\t" \
  "v_exp_f32 v181, v181\n\t" \
  "v_mfma_f32_32x32x16_bf16 v[192:207], v[232:235], v[168:171], 0\n\t" \
  "ds_read_b128 v[232:235], v252 offset:15744\n\t" \
  "v_cvt_pk_bf16_f32 v224, v176, v177\n\t" \
  "v_cvt_pk_bf16_f32 v225, v178, v179\n\t" \
  "v_exp_f32 v182, v182\n\t" \
  "buffer_load_dwordx4 v255, %[srd], %[t0] offen offset:2048 sc1 lds\n\t" \
  "v_exp_f32 v183, v183\n\t" \
  "v_exp_f32 v184, v184\n\t" \
  "v_exp_f32 v185, v185\n\t" \
  "v_mfma_f32_32x32x8_bf16 v[192:207], v[236:237], v[172:173], v[192:207]\n\t" \
  "ds_read_b32 v236, v253 offset:15744\n\t" \
  "v_cvt_pk_bf16_f32 v226, v180, v181\n\t" \
  "v_cvt_pk_bf16_f32 v227, v182, v183\n\t" \
  "v_exp_f32 v186, v186\n\t" \
  "v_exp_f32 v187, v187\n\t" \
  "v_exp_f32 v188, v188\n\t" \
  "v_exp_f32 v189, v189\n\t" \
  "s_waitcnt lgkmcnt(3)\n\t" \
  "v_mfma_f32_32x32x16_bf16 v[208:223], v[240:243], v[224:227], v[208:223]\n\t" \
  "v_cvt_pk_bf16_f32 v228, v184, v185\n\t" \
  "v_cvt_pk_bf16_f32 v229, v186, v187\n\t" \
  "v_exp_f32 v190, v190\n\t" \
  "buffer_load_dwordx4 v255, %[srd], %[t0] offen offset:3072 sc1 lds\n\t" \
  "v_exp_f32 v191, v191\n\t" \
  "v_cvt_pk_bf16_f32 v230, v188, v189\n\t" \
  "v_cvt_pk_bf16_f32 v231, v190, v191\n\t" \
  "v_exp_f32 v192, v192\n\t" \
  "v_exp_f32 v193, v193\n\t" \
  "s_waitcnt lgkmcnt(0)\n\t" \
  "v_mfma_f32_32x32x16_bf16 v[208:223], v[244:247], v[228:231], v[208:223]\n\t" \
  "v_exp_f32 v194, v194\n\t" \
  "v_exp_f32 v195, v195\n\t" \
  "v_exp_f32 v196, v196\n\t" \
  "v_exp_f32 v197, v197\n\t" \
  "v_mfma_f32_32x32x16_bf16 v[176:191], v[232:235], v[248:251], 0\n\t" \
  "v_cvt_pk_bf16_f32 v224, v192, v193\n\t" \
  "v_cvt_pk_bf16_f32 v225, v194, v195\n\t" \
  "v_exp_f32 v198, v198\n\t" \
  "s_mov_b32 m0, %[wom2]\n\t" \
  "s_nop 0\n\t" \
  "buffer_load_dwordx4 v255, %[srd], %[wos2] offen sc1 lds\n\t" \
  "v_exp_f32 v199, v199\n\t" \
  "v_exp_f32 v200, v200\n\t" \
  "v_exp_f32 v201, v201\n\t" \
  "v_mfma_f32_32x32x8_bf16 v[176:191], v[236:237], v[238:239], v[176:191]\n\t" \
  "v_cvt_pk_bf16_f32 v226, v196, v197\n\t" \
  "v_cvt_pk_bf16_f32 v227, v198, v199\n\t" \
  "v_exp_f32 v202, v202\n\t" \
  "v_exp_f32 v203, v203\n\t" \
  "v_exp_f32 v204, v204\n\t" \
  "v_exp_f32 v205, v205\n\t" \
  "v_mfma_f32_32x32x16_bf16 v[152:167], v[240:243], v[224:227], v[152:167]\n\t" \
  "ds_read_b128 v[240:243], v254 offset:15744\n\t" \
  "v_cvt_pk_bf16_f32 v228, v200, v201\n\t" \
  "v_cvt_pk_bf16_f32 v229, v202, v203\n\t" \
  "v_exp_f32 v206, v206\n\t" \
  "v_exp_f32 v207, v207\n\t" \
  "v_cvt_pk_bf16_f32 v230, v204, v205\n\t" \
  "v_cvt_pk_bf16_f32 v231, v206, v207\n\t" \
  "v_exp_f32 v176, v176\n\t" \
  "v_exp_f32 v177, v177\n\t" \
  "v_mfma_f32_32x32x16_bf16 v[152:167], v[244:247], v[228:231], v[152:167]\n\t" \
  "ds_read_b128 v[244:247], v254 offset:16416\n\t" \
  "v_exp_f32 v178, v178\n\t" \
  "v_exp_f32 v179, v179\n\t" \
  "v_exp_f32 v180, v180\n\t" \
  "v_exp_f32 v181, v181\n\t" \
  "v_mfma_f32_32x32x16_bf16 v[192:207], v[232:235], v[168:171], 0\n\t" \
  "v_cvt_pk_bf16_f32 v224, v176, v177\n\t" \
  "v_cvt_pk_bf16_f32 v225, v178, v179\n\t" \
  "v_exp_f32 v182, v182\n\t" \
  "v_exp_f32 v183, v183\n\t" \
  "v_exp_f32 v184, v184\n\t" \
  "v_exp_f32 v185, v185\n\t" \
  "v_mfma_f32_32x32x8_bf16 v[192:207], v[236:237], v[172:173], v[192:207]\n\t" \
  "v_cvt_pk_bf16_f32 v226, v180, v181\n\t" \
  "v_cvt_pk_bf16_f32 v227, v182, v183\n\t" \
  "v_exp_f32 v186, v186\n\t" \
  "v_exp_f32 v187, v187\n\t" \
  "v_exp_f32 v188, v188\n\t" \
  "v_exp_f32 v189, v189\n\t" \
  "s_waitcnt lgkmcnt(1)\n\t" \
  "v_mfma_f32_32x32x16_bf16 v[208:223], v[240:243], v[224:227], v[208:223]\n\t" \
  "v_cvt_pk_bf16_f32 v228, v184, v185\n\t" \
  "v_cvt_pk_bf16_f32 v229, v186, v187\n\t" \
  "v_exp_f32 v190, v190\n\t" \
  "v_exp_f32 v191, v191\n\t" \
  "v_cvt_pk_bf16_f32 v230, v188, v189\n\t" \
  "v_cvt_pk_bf16_f32 v231, v190, v191\n\t" \
  "v_exp_f32 v192, v192\n\t" \
  "v_exp_f32 v193, v193\n\t" \
  "s_waitcnt lgkmcnt(0)\n\t" \
  "v_mfma_f32_32x32x16_bf16 v[208:223], v[244:247], v[228:231], v[208:223]\n\t" \
  "v_exp_f32 v194, v194\n\t" \
  "v_exp_f32 v195, v195\n\t" \
  "v_exp_f32 v196, v196\n\t" \
  "v_exp_f32 v197, v197\n\t" \
  "v_cvt_pk_bf16_f32 v224, v192, v193\n\t" \
  "v_cvt_pk_bf16_f32 v225, v194, v195\n\t" \
  "v_exp_f32 v198, v198\n\t" \
  "v_exp_f32 v199, v199\n\t" \
  "v_exp_f32 v200, v200\n\t" \
  "v_exp_f32 v201, v201\n\t" \
  "v_cvt_pk_bf16_f32 v226, v196, v197\n\t" \
  "v_cvt_pk_bf16_f32 v227, v198, v199\n\t" \
  "v_exp_f32 v202, v202\n\t" \
  "v_exp_f32 v203, v203\n\t" \
  "v_exp_f32 v204, v204\n\t" \
  "v_exp_f32 v205, v205\n\t" \
  "v_mfma_f32_32x32x16_bf16 v[152:167], v[240:243], v[224:227], v[152:167]\n\t" \
  "v_cvt_pk_bf16_f32 v228, v200, v201\n\t" \
  "v_cvt_pk_bf16_f32 v229, v202, v203\n\t" \
  "v_exp_f32 v206, v206\n\t" \
  "v_exp_f32 v207, v207\n\t" \
  "v_cvt_pk_bf16_f32 v230, v204, v205\n\t" \
  "v_cvt_pk_bf16_f32 v231, v206, v207\n\t" \
  "s_nop 1\n\t" \
  "v_mfma_f32_32x32x16_bf16 v[152:167], v[244:247], v[228:231], v[152:167]\n\t" \
  "s_nop 11\n\t"
#define KV_DELAY 16
#define O_DELAY 4
#define H_DELAY 10
#define H1_DELAY 6
__global__ void __launch_bounds__(512, 2)
block_kernel(const uint16_t* __restrict__ x, const uint16_t* __restrict__ wq, const uint16_t* __restrict__ wk, const uint16_t* __restrict__ wv,
             const uint16_t* __restrict__ wo, const uint16_t* __restrict__ w1, const uint16_t* __restrict__ b1, const uint16_t* __restrict__ w2,
             const uint16_t* __restrict__ b2, uint16_t* __restrict__ out, char* __restrict__ ws) {
  extern __shared__ __attribute__((aligned(16))) char smem[];
  ST(0);
  u64 pc; unsigned len;
  asm volatile("s_getpc_b64 %0\n\tblk_pc_%=:\n\ts_mov_b32 %1, blk_end-blk_pc_%=" : "=s"(pc), "=s"(len));
  const int b = blockIdx.x;
  const int c = (b & 7) * 4 + (b >> 6), j = (b >> 3) & 7;
  const int tid = threadIdx.x, lane = tid & 63, w = __builtin_amdgcn_readfirstlane(tid >> 6);
  const int i16 = lane & 15, g = lane >> 4, q4 = i16 >> 2, p4 = lane & 3, r32 = lane & 31, hh = lane >> 5;
  const unsigned lds0 = (unsigned)(size_t)(LAS char*)smem;
  const unsigned l16 = lane * 16;
  const rsrc_t wsr = mk_rsrc(ws, (int)WS_END), xr_ = mk_rsrc(x, S * D * 2);
  const u32x4 ones4 = {0x3f803f80u, 0x3f803f80u, 0x3f803f80u, 0x3f803f80u};
  const bf16x8 ones = as_frag(ones4);

#define SB0() __builtin_amdgcn_sched_barrier(0)
  const int tg = w >= 4 ? w - 4 : (j >> 1);
  const unsigned xrow = (64 * c + 16 * tg) * (D * 2);
  LAS unsigned* sy = (LAS unsigned*)(smem + L_SY);
  if (w < 2) {
    const unsigned vo = (lane >> 2) * (D * 2) + (lane & 3) * 16, so = xrow + w * 640, ld = lds0 + L_XKV + w * 10240;
#define XK(p) DMA1("", xr_, vo, so, ld + (p) * 960, (p) * 64)
    XK(0); XK(1); XK(2); XK(3); XK(4); XK(5); XK(6); XK(7); XK(8); XK(9);
    const rsrc_t wr = mk_rsrc((j & 1) ? wv : wk, D * 20 * 2);
    const unsigned s2 = w * 12800, l2 = lds0 + L_WKV + w * 12800;
    DMA4("", wr, l16, s2, l2); DMA4("", wr, l16, s2 + 4096, l2 + 4096); DMA4("", wr, l16, s2 + 8192, l2 + 8192); DMA1("", wr, l16, s2 + 12288, l2 + 12288, 0);
  }
  if (tid >= 496) sy[tid - 496] = 0;
  const u32x2 ep = ld8(wsr, WS_EPOCH, 0);
  const int rot = ((c & 3) + j) % 5;
  BAR();
  u64 epoch = 0; unsigned tag_hi = 0, tag_lo = 0;
#define MAKE_TAG() do { u64 z = (((u64)(unsigned)__builtin_amdgcn_readfirstlane(ep[1]) << 32) | (unsigned)__builtin_amdgcn_readfirstlane(ep[0])); epoch = z;  \
    z += 0x9E3779B97F4A7C15ull; z = (z ^ (z >> 30)) * 0xBF58476D1CE4E5B9ull; z = (z ^ (z >> 27)) * 0x94D049BB133111EBull; z ^= z >> 31;                         \
    tag_hi = (unsigned)(z >> 32); tag_lo = (unsigned)z & 0xffff0000u; } while (0)
#define LDS_WAIT(i, v) do { _Pragma("nounroll") for (int spin = 0; spin < (SPIN_MAX << 4) && ((volatile LAS unsigned*)sy)[i] < (v); spin++) __builtin_amdgcn_s_sleep(1); asm volatile("" ::: "memory"); } while (0)
#define LDS_POST(i) do { asm volatile("s_waitcnt lgkmcnt(0)" ::: "memory"); if (lane == 0) __hip_atomic_fetch_add(sy + (i), 1u, __ATOMIC_RELAXED, __HIP_MEMORY_SCOPE_WORKGROUP); asm volatile("" ::: "memory"); } while (0)

  const char* wqb = smem + L_WQ + (16 * (g >> 1) + 4 * (g & 1) + q4) * 160 + 8 * p4;
  if (w < 2) {
    f32x4 acc[2] = {}, cs[2] = {}, gram = {}, sum = {};
    __builtin_amdgcn_s_setprio(2);
    WAIT_VM0();
    ST(1);
    const char* bp = smem + L_WKV + w * 12800 + (8 * g + q4) * 40 + 8 * p4;
    const char* ap = smem + L_XKV + w * 10240 + i16 * 64 + g * 16;
    {
      bf16x8 xb[10], wf[10][2];
#pragma unroll
      for (int s = 0; s < 10; s++) {
        xb[s] = *(const bf16x8*)(ap + s * 1024);
#pragma unroll
        for (int n = 0; n < 2; n++) wf[s][n] = frag2(tr_read(bp + s * 32 * 40 + n * 32), tr_read(bp + s * 32 * 40 + 4 * 40 + n * 32));
      }
      if ((j & 1) == 0) {
#pragma unroll
        for (int s = 0; s < 10; s++) {
#pragma unroll
          for (int n = 0; n < 2; n++) { acc[n] = mfma16(wf[s][n], xb[s], acc[n]); cs[n] = mfma16(wf[s][n], ones, cs[n]); }
          gram = mfma16(xb[s], xb[s], gram); sum = mfma16(ones, xb[s], sum);
        }
      } else {
#pragma unroll
        for (int s = 0; s < 10; s++) {
#pragma unroll
          for (int n = 0; n < 2; n++) { acc[n] = mfma16(xb[s], wf[s][n], acc[n]); cs[n] = mfma16(ones, wf[s][n], cs[n]); }
          gram = mfma16(xb[s], xb[s], gram); sum = mfma16(xb[s], ones, sum);
        }
      }
    }
    MAKE_TAG();
    char* pp = smem + L_XKV + 10240 + l16;
    if (w == 1) {
      *(f32x4*)(pp) = acc[0]; *(f32x4*)(pp + 1024) = acc[1]; *(f32x4*)(pp + 2048) = cs[0]; *(f32x4*)(pp + 3072) = cs[1]; *(f32x4*)(pp + 4096) = gram; *(f32x4*)(pp + 5120) = sum;
      LDS_POST(5);
      __builtin_amdgcn_s_setprio(0);
      f32x4 cq[5] = {};
      _Pragma("nounroll") for (int ch = 0; ch < 5; ch++) {
        LDS_WAIT(ch, 2);
#pragma unroll
        for (int s = 0; s < 4; s++)
#pragma unroll
          for (int n = 0; n < 5; n++)
            cq[n] = mfma16(frag2(tr_read(wqb + (4 * ch + s) * 5120 + n * 32), tr_read(wqb + (4 * ch + s) * 5120 + 8 * 160 + n * 32)), ones, cq[n]);
      }
      if (i16 == 0) {
#pragma unroll
        for (int n = 0; n < 5; n++) *(f32x4*)(smem + L_CS + (16 * n + 4 * g) * 4) = cq[n];
      }
      LDS_POST(8);
      ST(2);
    } else {
      LDS_WAIT(5, 1);
      acc[0] += *(const f32x4*)(pp); acc[1] += *(const f32x4*)(pp + 1024); cs[0] += *(const f32x4*)(pp + 2048); cs[1] += *(const f32x4*)(pp + 3072);
      gram += *(const f32x4*)(pp + 4096); sum += *(const f32x4*)(pp + 5120);
      const bool dg = q4 == g;
      const unsigned tile = WS_KV + (2 * c + (tg >> 1)) * KV_TILE;
      unsigned kn = 0;
      if ((j & 1) == 0) {
        float d = p4 == 0 ? gram[0] : p4 == 1 ? gram[1] : p4 == 2 ? gram[2] : gram[3];
        d = xrow_sum(dg ? d : 0.f);
        float mean = sum[0] * (1.f / D), rstd = __builtin_amdgcn_rsqf(fmaxf(d * (1.f / D) - mean * mean, 0.f) + EPS);
        unsigned o[4];
#pragma unroll
        for (int n = 0; n < 2; n++) {
          o[2 * n] = pk_bf16(rstd * (acc[n][0] - mean * cs[n][0]), rstd * (acc[n][1] - mean * cs[n][1]));
          o[2 * n + 1] = pk_bf16(rstd * (acc[n][2] - mean * cs[n][2]), rstd * (acc[n][3] - mean * cs[n][3]));
        }
        st8(u32x2{o[0], o[1]}, wsr, (i16 + 32 * (g >> 1)) * 16 + 8 * (g & 1), tile + (tg & 1) * 256);
        float n2 = sq2(o[0]) + sq2(o[1]);
        if (g == 0) {
          st4(o[2], wsr, i16 * 4, tile + 1024 + (tg & 1) * 64);
          st4(o[3], wsr, i16 * 4, tile + 1152 + (tg & 1) * 64);
          n2 += sq2(o[2]) + sq2(o[3]);
        }
        n2 = row16_max(xrow_sum(n2));
        kn = (__float_as_uint(n2) >> 16) + 1;
      } else {
        float mean[4], rstd[4];
#pragma unroll
        for (int r = 0; r < 4; r++) {
          float d = row16_sum(dg && p4 == r ? gram[r] : 0.f);
          mean[r] = sum[r] * (1.f / D);
          rstd[r] = __builtin_amdgcn_rsqf(fmaxf(d * (1.f / D) - mean[r] * mean[r], 0.f) + EPS);
        }
#pragma unroll
        for (int n = 0; n < 2; n++) {
          u32x2 o = {pk_bf16(rstd[0] * (acc[n][0] - mean[0] * cs[n][0]), rstd[1] * (acc[n][1] - mean[1] * cs[n][1])),
                     pk_bf16(rstd[2] * (acc[n][2] - mean[2] * cs[n][2]), rstd[3] * (acc[n][3] - mean[3] * cs[n][3]))};
          int d = 16 * n + i16, row = (g & 1) * 20 + d;
          if (d == 20) { unsigned cv = (g & 1) ? 0u : 0x3f803f80u; o = u32x2{cv, cv}; row = 40 + (g & 1); }
          if (d <= 20) st8(o, wsr, row * 16 + 8 * (g >> 1), tile + 1280 + (tg & 1) * 672);
        }
      }
      ST(2);
      WAIT_VM0();
      if (lane < 8) st8(u32x2{tag_lo | (kn & 0xffffu), tag_hi}, wsr, lane * 2048, WS_FKV + b * 8);
      ST(3);
      __builtin_amdgcn_s_sleep(KV_DELAY);
      unsigned km = 0;
      _Pragma("nounroll") for (int spin = 0; spin < SPIN_MAX; spin++) {
        asm volatile("" ::: "memory");
        u32x4 f0 = ld16(wsr, l16, WS_FKV + (b & 7) * 2048), f1 = ld16(wsr, l16 + 1024, WS_FKV + (b & 7) * 2048);
        unsigned bad = ((f0[1] ^ tag_hi) | (f0[3] ^ tag_hi) | (f1[1] ^ tag_hi) | (f1[3] ^ tag_hi)) |
                       (((f0[0] ^ tag_lo) | (f0[2] ^ tag_lo) | (f1[0] ^ tag_lo) | (f1[2] ^ tag_lo)) >> 16);
        km = umax3(umax3(f0[0], f0[2], f1[0]), f1[2], 0u) & 0xffffu;
        if (__all(bad == 0)) break;
        __builtin_amdgcn_s_sleep(4);
      }
      LDS_POST(7);
      ST(4);
      DMA4(" sc1", wsr, l16, WS_KV, lds0 + RING_TOP); DMA4(" sc1", wsr, l16, WS_KV + 4096, lds0 + RING_TOP + 4096); DMA4(" sc1", wsr, l16, WS_KV + 8192, lds0 + RING_TOP + 8192);
      DMA1(" sc1", wsr, l16, WS_KV + 12288, lds0 + RING_TOP + 12288, 0);
      float kmf = (float)km;
      kmf = xrow_max(row16_max(kmf));
      if (lane == 0) *(unsigned*)(smem + L_KMAX) = (unsigned)kmf << 16;
      __builtin_amdgcn_s_setprio(0);
    }
  } else if (w < 4) {
    unsigned junk;
    {
      const rsrc_t cr = __builtin_amdgcn_make_buffer_rsrc((void*)pc, 0, (int)len, 0x00020000);
      const unsigned v = lane * 128, s0 = (w - 2) * 24576;
      asm volatile("buffer_load_dword %0, %1, %2, %3 offen\n\tbuffer_load_dword %0, %1, %2, %4 offen\n\tbuffer_load_dword %0, %1, %2, %5 offen"
                   : "=&v"(junk) : "v"(v), "s"(cr), "s"(s0), "s"(s0 + 8192), "s"(s0 + 16384) : "memory");
    }
    const rsrc_t qr = mk_rsrc(wq, D * D * 2);
    unsigned vo[5];
#pragma unroll
    for (int u = 0; u < 5; u++) {
      unsigned q = u * 64 + lane, sl = (q * 205) >> 11, k = (sl & ~12u) | ((sl & 4) << 1) | ((sl & 8) >> 1);
      vo[u] = k * (D * 2) + (q - sl * 10) * 16;
    }
    _Pragma("nounroll") for (int ch = 0; ch < 5; ch++) {
      int cc = ch + rot; if (cc >= 5) cc -= 5;
      const unsigned so = (cc * 128 + (w - 2) * 64) * (D * 2) + 160 * j, ld = lds0 + L_WQ + (ch * 20 + (w - 2) * 10) * 1024;
#pragma unroll
      for (int u = 0; u < 5; u++) DMA1("", qr, vo[u], so, ld + u * 1024, 0);
#pragma unroll
      for (int u = 0; u < 5; u++) DMA1("", qr, vo[u], so + 32 * D * 2, ld + 5120 + u * 1024, 0);
      if (ch >= 2) { asm volatile("s_waitcnt vmcnt(20)" ::: "memory"); LDS_POST(ch - 2); }
    }
    asm volatile("s_waitcnt vmcnt(10)" ::: "memory"); LDS_POST(3);
    asm volatile("s_waitcnt vmcnt(0)" : "+v"(junk) :: "memory"); LDS_POST(4);
    ST(1);
    MAKE_TAG();
    LDS_WAIT(7, 1);
    const unsigned o = 13312 + (w - 2) * 14336;
    DMA4(" sc1", wsr, l16, WS_KV + o, lds0 + RING_TOP + o); DMA4(" sc1", wsr, l16, WS_KV + o + 4096, lds0 + RING_TOP + o + 4096);
    DMA4(" sc1", wsr, l16, WS_KV + o + 8192, lds0 + RING_TOP + o + 8192); DMA2(" sc1", wsr, l16, WS_KV + o + 12288, lds0 + RING_TOP + o + 12288);
  } else {
    f32x4 acc[5] = {}, gram = {}, sum = {};
    u32x4 xr[20];
    const unsigned vo = (lane >> 3) * (D * 2) + (lane & 7) * 16;
#pragma unroll
    for (int ch = 0; ch < 5; ch++) {
      int cc = ch + rot; if (cc >= 5) cc -= 5;
      const unsigned so = xrow + cc * 256;
      xr[4 * ch] = in16(xr_, vo, so); xr[4 * ch + 1] = in16(xr_, vo, so + 8 * D * 2); xr[4 * ch + 2] = in16(xr_, vo + 128, so); xr[4 * ch + 3] = in16(xr_, vo + 128, so + 8 * D * 2);
    }
    char* stg = smem + L_QS + tg * 2560;
    const int st_w = (lane >> 3) * 128 + (((lane & 7) ^ (lane >> 4)) << 4);
    const int st_r = i16 * 128 + ((g ^ (i16 >> 1)) << 4);
#pragma unroll
    for (int u = 0; u < 10; u++) {
      *(u32x4*)(stg + st_w) = xr[2 * u]; *(u32x4*)(stg + (st_w ^ 64) + 1024) = xr[2 * u + 1];
      xr[2 * u] = *(const u32x4*)(stg + st_r); xr[2 * u + 1] = *(const u32x4*)(stg + (st_r ^ 64));
      if (u > 0) {
#pragma unroll
        for (int s = 2 * u - 2; s < 2 * u; s++) { gram = mfma16(as_frag(xr[s]), as_frag(xr[s]), gram); sum = mfma16(ones, as_frag(xr[s]), sum); }
        PIN(gram); PIN(sum);
      }
      SB0();
    }
#pragma unroll
    for (int s = 18; s < 20; s++) { gram = mfma16(as_frag(xr[s]), as_frag(xr[s]), gram); sum = mfma16(ones, as_frag(xr[s]), sum); }
    MAKE_TAG();
    float d = p4 == 0 ? gram[0] : p4 == 1 ? gram[1] : p4 == 2 ? gram[2] : gram[3];
    d = xrow_sum(q4 == g ? d : 0.f);
    float mean = sum[0] * (1.f / D), rs = __builtin_amdgcn_rsqf(fmaxf(d * (1.f / D) - mean * mean, 0.f) + EPS) * QSCALE;
    PIN(mean); PIN(rs);
    ST(1);
#pragma unroll
    for (int ch = 0; ch < 5; ch++) {
      LDS_WAIT(ch, 2);
      bf16x8 wf[4][5];
#pragma unroll
      for (int s = 0; s < 4; s++)
#pragma unroll
        for (int n = 0; n < 5; n++) wf[s][n] = frag2(tr_read(wqb + (4 * ch + s) * 5120 + n * 32), tr_read(wqb + (4 * ch + s) * 5120 + 8 * 160 + n * 32));
#pragma unroll
      for (int s = 0; s < 4; s++)
#pragma unroll
        for (int n = 0; n < 5; n++) acc[n] = mfma16(wf[s][n], as_frag(xr[4 * ch + s]), acc[n]);
    }
    ST(2);
    LDS_WAIT(8, 1);
    char* qp = smem + L_QS + (16 * tg + i16) * 160 + 8 * g;
#pragma unroll
    for (int n = 0; n < 5; n++) {
      f32x4 cs = *(const f32x4*)(smem + L_CS + (16 * n + 4 * g) * 4);
      *(u32x2*)(qp + 32 * n) = u32x2{pk_bf16(rs * (acc[n][0] - mean * cs[0]), rs * (acc[n][1] - mean * cs[1])),
                                     pk_bf16(rs * (acc[n][2] - mean * cs[2]), rs * (acc[n][3] - mean * cs[3]))};
    }
    LDS_POST(9);
    ST(3);
  }

  const int hl = w & 3, kh = w >> 2;
  bf16x8 q1[2]; u32x2 q2[2];
  f32x16 o0, o1;
  unsigned w2touch;
  u32x32 w1a, w1b, w1c; u32x16 w1d; u32x8 w1e;
  {
    float qn[2];
    LDS_WAIT(9, 4);
#pragma unroll
    for (int t = 0; t < 2; t++) {
      const char* qp = smem + L_QS + (32 * (t ^ kh) + r32) * 160 + 40 * hl;
      u32x2 qa = *(const u32x2*)(qp + 16 * hh), qb = *(const u32x2*)(qp + 16 * hh + 8);
      unsigned qc = *(const unsigned*)(qp + 32 + 4 * hh);
      qn[t] = sq2(qa[0]) + sq2(qa[1]) + sq2(qb[0]) + sq2(qb[1]) + sq2(qc);
      qn[t] += swap32(qn[t], hh);
      q1[t] = as_frag(u32x4{qa[0], qa[1], qb[0], qb[1]});
      q2[t][0] = qc;
      PIN(qn[t]);
    }
    const unsigned tb = lds0 + RING_TOP + kh * KV_TILE;
    unsigned ak1 = tb + l16, ak2 = tb + 1024 + lane * 4, av = tb + 1280 + (r32 < 20 ? hh * 20 + r32 : (r32 == 20 ? 40 : 41)) * 16;
    u32x2 k2 = {0u, hh == 0 ? 0x00003f80u : 0u};
    PIN(ak1); PIN(ak2); PIN(av); PIN(k2);
    const int task = b + 256 * (w - 3);
    const uint16_t* sw; unsigned ld, KS, ks, cb, d0, A, B = 16, C1 = 8, C0 = 4;
    if (task < 680) {
      const bool iswo = task < 200;
      const unsigned u = iswo ? (task < 0 ? 0 : task) : task - 200, ncb = iswo ? 10 : 24;
      sw = iswo ? wo : w1; d0 = iswo ? WS_WOF : WS_W1F; ld = iswo ? D * 2 : F * 2; KS = 20; ks = u / ncb; cb = u - ks * ncb;
      const unsigned P = ks / 5, uu = ks - 5 * P;
      if (uu < 4) { A = 160 * P + 80 * (uu >> 1) + (iswo ? 40 : 32) * (uu & 1); if (iswo) { B = 8; C1 = 20; } }
      else { A = 160 * P + (iswo ? 16 : 64); B = 80; if (iswo) { C1 = 40; C0 = 20; } }
    } else {
      const unsigned u = task - 680;
      sw = w2; d0 = WS_W2F; ld = D * 2; KS = 48; ks = u / 10; cb = u - ks * 10;
      const unsigned jj = ks / 6, ss = ks - 6 * jj;
      if (ss < 4) A = 192 * jj + 48 * ss; else { A = 192 * jj + 96 * (ss - 4) + 32; B = 48; }
    }
    const rsrc_t ppsrd = mk_rsrc(sw, KS * 32 * ld);
    unsigned ppv = (C0 * hh + ((lane >> 3) & 3)) * ld + (lane & 7) * 16, ppa = lds0 + L_PP + (w - 3) * 4096 + (4 * g + q4) * 128 + 8 * p4;
    unsigned ppd = d0 + (4 * cb * KS + ks) * 1024;
    const unsigned ppk = (task >= 0 && task < 1160) ? KS * 1024 : 0, pps0 = A * ld + 128 * cb;
    PIN(ppv); PIN(ppa);
    WAIT_VM0();
    BAR();
    ST(5);
    const float kmax2 = *(const float*)(smem + L_KMAX);
#pragma unroll
    for (int t = 0; t < 2; t++) q2[t][1] = hh == 0 ? pk_bf16(-__builtin_amdgcn_sqrtf(qn[t] * kmax2) * 1.0078125f, 0.f) : 0u;
    unsigned t0, clr = WS_OF + (b * 48 + 6 * w) * 1024; u64 t1;
    asm volatile(ATT_ALL
      : "=&{v[208:223]}"(o0), "=&{v[152:167]}"(o1), "+{v[236:237]}"(k2), "+{v252}"(ak1), "+{v253}"(ak2), "+{v254}"(av),
        "=&{v[32:63]}"(w1a), "=&{v[64:95]}"(w1b), "=&{v[96:127]}"(w1c), "=&{v[128:143]}"(w1d), "=&{v[144:151]}"(w1e),
        [t0] "=&s"(t0), [t1] "=&s"(t1), [clr] "+s"(clr), [ppd] "+s"(ppd), "+{v174}"(ppv), "+{v175}"(ppa)
      : "{v[248:251]}"(q1[0]), "{v[238:239]}"(q2[0]), "{v[168:171]}"(q1[1]), "{v[172:173]}"(q2[1]), "{v255}"(l16), [srd] "s"(wsr),
        [kvs] "s"(WS_KV + 16 * KV_TILE + w * 12288), [kvm] "s"(lds0 + RING0 + w * 12288),
        [kv3s] "s"(WS_KV + 16 * KV_TILE + 98304 + w * 4096), [kv3m] "s"(lds0 + RING0 + 98304 + w * 4096),
        [wid] "s"(w), [tlo] "s"(tag_lo), [thi] "s"(tag_hi), [tag] "s"(((u64)tag_hi << 32) | tag_lo),
        [fws] "s"(WS_FW + b * 8), [fwr] "s"(WS_FW + (b & 7) * 2048), [w1s] "s"(WS_W1F + ((12 * j + 3 * hl) * 20 + 10 * kh) * 1024),
        [wos] "s"(WS_WOF + j * 102400 + w * 12288), [wom] "s"(lds0 + L_WO + w * 12288),
        [wos2] "s"(WS_WOF + j * 102400 + 98304 + hl * 1024), [wom2] "s"(lds0 + L_WO + 98304 + hl * 1024),
        [ppsrd] "s"(ppsrd), [ppk] "s"(ppk), [ppm] "s"(lds0 + L_PP + (w - 3) * 4096), [pps0] "s"(pps0), [pps1] "s"(pps0 + C1 * ld), [pps2] "s"(pps0 + B * ld),
        [pps3] "s"(pps0 + (B + C1) * ld), [w2t] "s"(WS_W2F + j * 245760 + (c & 3) * 61440 + w * 8192)
      : "memory", "scc", "vcc", "m0",
        "v176", "v177", "v178", "v179", "v180", "v181", "v182", "v183", "v184", "v185", "v186", "v187", "v188", "v189", "v190", "v191",
        "v192", "v193", "v194", "v195", "v196", "v197", "v198", "v199", "v200", "v201", "v202", "v203", "v204", "v205", "v206", "v207",
        "v224", "v225", "v226", "v227", "v228", "v229", "v230", "v231", "v232", "v233", "v234", "v235", "v240", "v241", "v242", "v243", "v244", "v245", "v246", "v247");
    w2touch = ppa;
  }
  ST(9);
  unsigned l16b = l16;
  PIN(l16b);
#define l16 l16b
#define lane ((int)(l16b >> 4))
#define i16 ((int)(l16b >> 4) & 15)
#define g ((int)(l16b >> 8))
#define r32 ((int)(l16b >> 4) & 31)
#define hh ((int)(l16b >> 9))
#define HINT_SEND(e) do { if (w == 0 && lane == 0) st4(tag_hi, wsr, 0, WS_HINT + (e) * 32768 + (c * 8 + j) * 128); } while (0)
#define HINT_WAIT(e, m0, nm) do { _Pragma("nounroll") for (int spin = 0; spin < SPIN_MAX; spin++) { asm volatile("" ::: "memory");                         \
    unsigned f = __builtin_amdgcn_raw_buffer_load_b32(wsr, (lane & ((nm) - 1)) * 128, WS_HINT + (e) * 32768 + (c * 8 + (m0)) * 128, 16);                    \
    if (__all(f == tag_hi)) break; __builtin_amdgcn_s_sleep(2); } } while (0)
  u32x2 xres[3];
#pragma unroll
  for (int m = 0; m < 3; m++) xres[m] = in8(xr_, i16 * (D * 2) + g * 8, (64 * c + 16 * hl) * (D * 2) + (80 * j + (m < 2 ? 32 * kh + 16 * m : 64)) * 2);
  {
    const f32x16 snd = o1;
    char* xb = smem + L_XO + (w * 3) * 1024 + l16;
#pragma unroll
    for (int q = 0; q < 3; q++) *(f32x4*)(xb + q * 1024) = f32x4{snd[4 * q], snd[4 * q + 1], snd[4 * q + 2], snd[4 * q + 3]};
    asm volatile("s_waitcnt vmcnt(0)" : "+{v[32:63]}"(w1a), "+{v[64:95]}"(w1b), "+{v[96:127]}"(w1c), "+{v[128:143]}"(w1d), "+{v[144:151]}"(w1e), "+{v175}"(w2touch) :: "memory");
    BAR();
    HINT_SEND(0);
    const char* yb = smem + L_XO + ((w ^ 4) * 3) * 1024 + l16;
#pragma unroll
    for (int q = 0; q < 3; q++) { f32x4 v = *(const f32x4*)(yb + q * 1024); o0[4 * q] += v[0]; o0[4 * q + 1] += v[1]; o0[4 * q + 2] += v[2]; o0[4 * q + 3] += v[3]; }
  }
  float l = o0[8]; { float ls = swap32(l, hh); l = hh ? l : ls; }
  if (!__all(l >= 8.6736174e-19f  )) {
    const u32x2 q2z = {q2[0][0], 0u}; const bf16x8 q1f = q1[0];
    const int off_v = 1280 + (r32 < 20 ? hh * 20 + r32 : (r32 == 20 ? 40 : 41)) * 16;
    float m = -INFINITY;
    o0 = f32x16{};
#pragma unroll 1
    for (int t = 0; t < 64; t++) {
      const unsigned tp = WS_KV + t * KV_TILE;
      u32x4 k1 = ld16(wsr, l16, tp); unsigned k2 = __builtin_amdgcn_raw_buffer_load_b32(wsr, 1024 + lane * 4, tp, 16);
      u32x4 v0 = ld16(wsr, off_v, tp), v1 = ld16(wsr, off_v + 672, tp);
      f32x16 s = mfma32(as_frag(k1), q1f, f32x16{});
      s = mfma32k8(u32x2{k2, 0u}, q2z, s);
      float tm = s[0];
#pragma unroll
      for (int r = 1; r < 16; r++) tm = fmaxf(tm, s[r]);
      tm = fmaxf(tm, swap32(tm, hh));
      float mn = fmaxf(m, tm), alpha = __builtin_amdgcn_exp2f(m - mn);
      m = mn;
#pragma unroll
      for (int r = 0; r < 16; r++) o0[r] *= alpha;
      u32x4 p0, p1;
#pragma unroll
      for (int r = 0; r < 4; r++) {
        p0[r] = pk_bf16(__builtin_amdgcn_exp2f(s[2 * r] - m), __builtin_amdgcn_exp2f(s[2 * r + 1] - m));
        p1[r] = pk_bf16(__builtin_amdgcn_exp2f(s[8 + 2 * r] - m), __builtin_amdgcn_exp2f(s[8 + 2 * r + 1] - m));
      }
      o0 = mfma32(as_frag(v0), as_frag(p0), o0);
      o0 = mfma32(as_frag(v1), as_frag(p1), o0);
    }
    l = o0[8]; { float ls = swap32(l, hh); l = hh ? l : ls; }
  }
  {
    const float rl = __builtin_amdgcn_rcpf(l);
    const unsigned os = WS_OF + (4 * c + 2 * kh) * 20480 + j * 2560;
    st16(u32x4{pk_out(o0[0] * rl, o0[1] * rl), pk_out(o0[2] * rl, o0[3] * rl), pk_out(o0[4] * rl, o0[5] * rl), pk_out(o0[6] * rl, o0[7] * rl)},
         wsr, (r32 >> 4) * 20480 + hh * 256 + i16 * 16, os + hl * 512);
    if (!hh) st8(u32x2{pk_out(o0[8] * rl, o0[9] * rl), pk_out(o0[10] * rl, o0[11] * rl)}, wsr, (r32 >> 4) * 20480 + i16 * 8, os + 2048 + hl * 128);
  }
  ST(10);
  if (b == 0 && w == 0 && lane == 0) st8(u32x2{(unsigned)(epoch + 1), (unsigned)((epoch + 1) >> 32)}, wsr, WS_EPOCH, 0);
#define SUB4(v, i) __builtin_shufflevector(v, v, 4 * (i), 4 * (i) + 1, 4 * (i) + 2, 4 * (i) + 3)
  const u32x4 w1f[3][10] = {
    {SUB4(w1a, 0), SUB4(w1a, 1), SUB4(w1a, 2), SUB4(w1a, 3), SUB4(w1a, 4), SUB4(w1a, 5), SUB4(w1a, 6), SUB4(w1a, 7), SUB4(w1b, 0), SUB4(w1b, 1)},
    {SUB4(w1b, 2), SUB4(w1b, 3), SUB4(w1b, 4), SUB4(w1b, 5), SUB4(w1b, 6), SUB4(w1b, 7), SUB4(w1c, 0), SUB4(w1c, 1), SUB4(w1c, 2), SUB4(w1c, 3)},
    {SUB4(w1c, 4), SUB4(w1c, 5), SUB4(w1c, 6), SUB4(w1c, 7), SUB4(w1d, 0), SUB4(w1d, 1), SUB4(w1d, 2), SUB4(w1d, 3), SUB4(w1e, 0), SUB4(w1e, 1)}};

  u32x2 hres[3];
  f32x4 cs1[3] = {};
  {
#pragma unroll
    for (int n = 0; n < 3; n++)
#pragma unroll
      for (int s = 0; s < 10; s++) cs1[n] = mfma16(as_frag(w1f[n][s]), ones, cs1[n]);
    PIN(cs1[0]); PIN(cs1[1]); PIN(cs1[2]);
    HINT_WAIT(0, 4 * kh, 4);
    ST(6);
    const unsigned abase = WS_OF + (4 * c + hl) * 20480 + kh * 10240, l8 = l16 >> 1;
    const char* bp = smem + L_WO + (10 * kh + 40 * kh) * 1024 + l16;
    unsigned bp2o = L_WO + (10 * kh + 40 - 40 * kh) * 1024 + l16, bp3o = L_WO + (10 * kh + 80) * 1024 + l16; PIN(bp2o); PIN(bp3o);
    const char* bp2 = smem + bp2o; const char* bp3 = smem + bp3o;
#define WOR(n, s) *(const bf16x8*)((n) < 2 ? bp + ((n) * 20 + (s)) * 1024 : (n) < 4 ? bp2 + (((n) - 2) * 20 + (s)) * 1024 : bp3 + (s) * 1024)
#define LD_HALVES(base_a, base_b) ({ u32x2 lo_ = ld8(wsr, l8, base_a), hi_ = ld8(wsr, l8, base_b); u32x4{lo_[0], lo_[1], hi_[0], hi_[1]}; })
    f32x4 a2[5];
    _Pragma("nounroll") for (int spin = 0; spin < SPIN_MAX; spin++) {
      asm volatile("" ::: "memory");
      u32x4 af[10];
#pragma unroll
      for (int p = 0; p < 2; p++) {
        af[5 * p] = ld16(wsr, l16, abase + p * 5120); af[5 * p + 1] = ld16(wsr, l16, abase + p * 5120 + 1024);
        af[5 * p + 2] = ld16(wsr, l16, abase + p * 5120 + 2560); af[5 * p + 3] = ld16(wsr, l16, abase + p * 5120 + 3584);
        af[5 * p + 4] = LD_HALVES(abase + p * 5120 + 2048, abase + p * 5120 + 4608);
      }
      bf16x8 bf[2][5];
#pragma unroll
      for (int n = 0; n < 5; n++) { bf[0][n] = WOR(n, 0); a2[n] = f32x4{}; }
      unsigned m = 0;
#pragma unroll
      for (int s = 0; s < 10; s++) {
        if (s + 1 < 10) {
#pragma unroll
          for (int n = 0; n < 5; n++) bf[(s + 1) & 1][n] = WOR(n, s + 1);
        }
        SB0();
#pragma unroll
        for (int n = 0; n < 5; n++) a2[n] = mfma16(bf[s & 1][n], as_frag(af[s]), a2[n]);
        m = umax4(m, af[s]);
        PIN(a2[0]); PIN(a2[1]); PIN(a2[2]); PIN(a2[3]); PIN(a2[4]); PIN(m);
        SB0();
      }
      if (__all(m != SENT)) break;
    }
    ST(11);
    char* xb = smem + L_X2 + (w * 3) * 1024 + l16;
    *(f32x4*)(xb) = a2[2]; *(f32x4*)(xb + 1024) = a2[3];
    if (kh) *(f32x4*)(xb + 2048) = a2[4];
    BAR();
    HINT_SEND(1);
    const char* yb = smem + L_X2 + ((w ^ 4) * 3) * 1024 + l16;
    float s1 = 0.f, s2 = 0.f;
    const unsigned hs = WS_HF + (4 * c + hl) * 20480 + j * 2560;
#pragma unroll
    for (int m = 0; m < 3; m++) {
      if (m == 2 && kh) break;
      f32x4 v = (m < 2 ? a2[m] : a2[4]) + *(const f32x4*)(yb + m * 1024);
      unsigned a0 = pk_bf16(v[0], v[1]), a1 = pk_bf16(v[2], v[3]);
      hres[m] = u32x2{pk_out(bf_lo(xres[m][0]) + bf_lo(a0), bf_hi(xres[m][0]) + bf_hi(a0)), pk_out(bf_lo(xres[m][1]) + bf_lo(a1), bf_hi(xres[m][1]) + bf_hi(a1))};
      if (m == 1) st16(u32x4{hres[0][0], hres[0][1], hres[1][0], hres[1][1]}, wsr, l16, hs + kh * 1024);
      if (m == 2) st8(hres[2], wsr, l8, hs + 2048);
      s1 += (bf_lo(hres[m][0]) + bf_hi(hres[m][0])) + (bf_lo(hres[m][1]) + bf_hi(hres[m][1]));
      s2 += sq2(hres[m][0]) + sq2(hres[m][1]);
    }
    s1 = xrow_sum(s1); s2 = xrow_sum(s2);
    unsigned u1 = __float_as_uint(s1), u2 = __float_as_uint(s2);
    if (g == 0) st8(u32x2{u1 < SENT ? u1 : SENT - 1, u2 < SENT ? u2 : SENT - 1}, wsr, l8, WS_ST + (4 * c + hl) * 2048 + (2 * j + kh) * 128);
  }
  ST(12);
  PIN(l16b);

  u32x4 w2f[5][6];
  {
    u32x2 bv[3];
    const rsrc_t br = mk_rsrc(b1, F * 2);
#pragma unroll
    for (int n = 0; n < 3; n++) bv[n] = in8(br, g * 8 + n * 32, (192 * j + 48 * hl) * 2);
    HINT_WAIT(1, 0, 8);
    ST(7);
    const unsigned hs = WS_HF + (4 * c * 20 + 10 * w) * 1024, hd = lds0 + L_H + 10 * w * 1024;
    _Pragma("nounroll") for (int spin = 0; spin < SPIN_MAX; spin++) {
      DMA4(" sc1", wsr, l16, hs, hd); DMA4(" sc1", wsr, l16, hs + 4096, hd + 4096); DMA2(" sc1", wsr, l16, hs + 8192, hd + 8192);
      u32x4 sa = {}, sb = {};
      if (kh == 0) {
        const unsigned l8 = l16 >> 1, ss = WS_ST + (4 * c + hl) * 2048;
        sa = LD_HALVES(ss, ss + 512); sb = LD_HALVES(ss + 1024, ss + 1536);
      }
      WAIT_VM0();
      unsigned m = umax4(umax4(0u, sa), sb);
#pragma unroll
      for (int s = 0; s < 10; s++) m = umax4(m, *(const u32x4*)(smem + L_H + (10 * w + s) * 1024 + l16));
      if (kh == 0) {
        float s1 = (__uint_as_float(sa[0]) + __uint_as_float(sa[2])) + (__uint_as_float(sb[0]) + __uint_as_float(sb[2]));
        float s2 = (__uint_as_float(sa[1]) + __uint_as_float(sa[3])) + (__uint_as_float(sb[1]) + __uint_as_float(sb[3]));
        s1 = xrow_sum(s1); s2 = xrow_sum(s2);
        float mean = s1 * (1.f / D), rstd = __builtin_amdgcn_rsqf(fmaxf(s2 * (1.f / D) - mean * mean, 0.f) + EPS);
        if (g == 0) *(f32x2*)(smem + L_MR + (16 * hl + i16) * 8) = f32x2{mean, rstd};
      }
      if (__all(m != SENT)) break;
    }
    BAR();
    ST(13);
    f32x4 a3[4][3] = {};
    const char* ap = smem + L_H + (10 * kh + 40 * kh) * 1024 + l16;
    unsigned ap2o = L_H + (10 * kh + 40 - 40 * kh) * 1024 + l16; PIN(ap2o);
    const char* ap2 = smem + ap2o;
    unsigned apho = L_H + (10 * kh + 40 * kh) * 1024 + (l16 >> 1), aph2o = L_H + (10 * kh + 40 - 40 * kh) * 1024 + (l16 >> 1); PIN(apho); PIN(aph2o);
    const char* aph = smem + apho; const char* aph2 = smem + aph2o;
#define HOFF(t, s) (((t) & 1) * 20480 + ((s) / 5) * 5120 + ((s) % 5 >> 1) * 2560 + ((s) % 5 & 1) * 1024)
#define HFR(t, s) ((s) % 5 < 4 ? *(const bf16x8*)(((t) < 2 ? ap : ap2) + HOFF(t, s))                                                                 \
    : frag2(*(const s16x4*)(((t) < 2 ? aph : aph2) + ((t) & 1) * 20480 + ((s) / 5) * 5120 + 2048), *(const s16x4*)(((t) < 2 ? aph : aph2) + ((t) & 1) * 20480 + ((s) / 5) * 5120 + 4608)))
    const unsigned w2base = WS_W2F + (5 * j * 48 + 6 * w) * 1024;
    bf16x8 hf[2][4];
#pragma unroll
    for (int t = 0; t < 4; t++) hf[0][t] = HFR(t, 0);
#pragma unroll
    for (int s = 0; s < 10; s++) {
      if (s + 1 < 10) {
#pragma unroll
        for (int t = 0; t < 4; t++) hf[(s + 1) & 1][t] = HFR(t, s + 1);
      }
      SB0();
#pragma unroll
      for (int t = 0; t < 4; t++) {
#pragma unroll
        for (int n = 0; n < 3; n++) a3[t][n] = mfma16(as_frag(w1f[n][s]), hf[s & 1][t], a3[t][n]);
        PIN(a3[t][0]); PIN(a3[t][1]); PIN(a3[t][2]);
      }
      SB0();
#pragma unroll
      for (int u = 3 * s; u < 3 * s + 3; u++) w2f[u % 5][u / 5] = ld16(wsr, l16, w2base + ((u % 5 < 4 ? (u % 5) ^ (2 * kh) : 4) * 48 + u / 5) * 1024);
    }
    ST(18);
    char* xb = smem + L_X3 + (w * 6) * 1024 + l16;
    f32x2 mr[4];
#pragma unroll
    for (int t = 0; t < 4; t++) mr[t] = *(const f32x2*)(smem + L_MR + (16 * (t ^ (2 * kh)) + i16) * 8);
#pragma unroll
    for (int t = 2; t < 4; t++)
#pragma unroll
      for (int n = 0; n < 3; n++) *(f32x4*)(xb + ((t - 2) * 3 + n) * 1024) = a3[t][n] - mr[t][0] * cs1[n];
#pragma unroll
    for (int t = 0; t < 2; t++)
#pragma unroll
      for (int n = 0; n < 3; n++) { a3[t][n] -= mr[t][0] * cs1[n]; PIN(a3[t][n]); }
    BAR();
    HINT_SEND(2);
    const char* yb = smem + L_X3 + ((w ^ 4) * 6) * 1024 + l16;
#pragma unroll
    for (int t = 0; t < 2; t++) {
      u32x2 ov[3];
#pragma unroll
      for (int n = 0; n < 3; n++) {
        f32x4 v = a3[t][n] + *(const f32x4*)(yb + (t * 3 + n) * 1024);
        float z0 = fmaxf(mr[t][1] * v[0] + bf_lo(bv[n][0]), 0.f), z1 = fmaxf(mr[t][1] * v[1] + bf_hi(bv[n][0]), 0.f);
        float z2 = fmaxf(mr[t][1] * v[2] + bf_lo(bv[n][1]), 0.f), z3 = fmaxf(mr[t][1] * v[3] + bf_hi(bv[n][1]), 0.f);
        ov[n] = u32x2{pk_out(z0 * z0, z1 * z1), pk_out(z2 * z2, z3 * z3)};
      }
      const unsigned os = WS_H1F + (4 * c + 2 * kh + t) * 49152 + j * 6144 + hl * 1536;
      st16(u32x4{ov[0][0], ov[0][1], ov[1][0], ov[1][1]}, wsr, l16, os);
      st8(ov[2], wsr, l16 >> 1, os + 1024);
    }
    BAR();
  }
  ST(14);
  PIN(l16b);

  {
    u32x2 bv[3];
    const rsrc_t br = mk_rsrc(b2, D * 2);
#pragma unroll
    for (int m = 0; m < 3; m++) bv[m] = in8(br, g * 8, (80 * j + (m < 2 ? 32 * kh + 16 * m : 64)) * 2);
    const unsigned abase = WS_H1F + 4 * c * 49152 + w * 6144, l8 = l16 >> 1;
    char* pb = smem + w * 20480 + kh * 2048 + l16; char* pb2 = smem + w * 20480 + 2048 - kh * 2048 + l16; char* pb3 = smem + w * 20480 + 4096 + l16;
    HINT_WAIT(2, w, 1);
    ST(8);
    u32x4 af[4][6];
#define H1_LOAD(t) do { _Pragma("unroll") for (int s = 0; s < 4; s++) af[t][s] = ld16(wsr, l16, abase + (t) * 49152 + s * 1536);                  \
      af[t][4] = LD_HALVES(abase + (t) * 49152 + 1024, abase + (t) * 49152 + 2560); af[t][5] = LD_HALVES(abase + (t) * 49152 + 4096, abase + (t) * 49152 + 5632); } while (0)
    H1_LOAD(0); H1_LOAD(1); H1_LOAD(2);
#pragma unroll
    for (int t = 0; t < 4; t++) {
      _Pragma("nounroll") for (int spin = 0; spin < SPIN_MAX; spin++) {
        unsigned m = 0;
#pragma unroll
        for (int s = 0; s < 6; s++) m = umax4(m, af[t][s]);
        if (__all(m != SENT)) break;
        asm volatile("" ::: "memory");
        H1_LOAD(t);
      }
      if (t == 0) ST(15);
      f32x4 a4[5] = {};
      if (t == hl) {
#pragma unroll
        for (int m = 0; m < 3; m++) {
          f32x4 v = {bf_lo(hres[m][0]) + bf_lo(bv[m][0]), bf_hi(hres[m][0]) + bf_hi(bv[m][0]), bf_lo(hres[m][1]) + bf_lo(bv[m][1]), bf_hi(hres[m][1]) + bf_hi(bv[m][1])};
          if (m < 2) a4[m] = v; else if (kh == 0) a4[4] = v;
        }
      }
#pragma unroll
      for (int s = 0; s < 6; s++)
#pragma unroll
        for (int n = 0; n < 5; n++) a4[n] = mfma16(as_frag(w2f[n][s]), as_frag(af[t][s]), a4[n]);
      if (t == 0) { PIN(a4[0]); PIN(a4[1]); PIN(a4[2]); PIN(a4[3]); PIN(a4[4]); asm volatile("" ::: "memory"); SB0(); H1_LOAD(3); SB0(); }
#pragma unroll
      for (int n = 0; n < 5; n++) *(f32x4*)((n < 2 ? pb + n * 1024 : n < 4 ? pb2 + (n - 2) * 1024 : pb3) + t * 5120) = a4[n];
    }
    ST(16);
    BAR();
    PIN(l16b);
    const rsrc_t orr = mk_rsrc(out, S * D * 2);
#pragma unroll
    for (int it = 0; it < 3; it++) {
      const int tile = w + 8 * it;
      if (tile < 20) {
        f32x4 v = {};
#pragma unroll
        for (int u = 0; u < 8; u++) v += *(const f32x4*)(smem + u * 20480 + tile * 1024 + l16);
        const int t = tile / 5, n = tile - 5 * t;
        __builtin_amdgcn_raw_buffer_store_b64(u32x2{pk_bf16(v[0], v[1]), pk_bf16(v[2], v[3])}, orr, i16 * (D * 2) + g * 8, (64 * c + 16 * t) * (D * 2) + (80 * j + 16 * n) * 2, 0);
      }
    }
  }
  ST(17);
  asm volatile("blk_end:");
#undef l16
#undef lane
#undef i16
#undef g
#undef r32
#undef hh
}

extern "C" void kernel_launch(void* const* d_in, const int* in_sizes, int n_in, void* d_out, int out_size, void* d_ws,
                              size_t ws_size, hipStream_t stream) {
  (void)hipFuncSetAttribute((const void*)block_kernel, hipFuncAttributeMaxDynamicSharedMemorySize, LDS_BYTES);
  block_kernel<<<256, 512, LDS_BYTES, stream>>>((const uint16_t*)d_in[0], (const uint16_t*)d_in[1], (const uint16_t*)d_in[2], (const uint16_t*)d_in[3],
                                                (const uint16_t*)d_in[4], (const uint16_t*)d_in[6], (const uint16_t*)d_in[7], (const uint16_t*)d_in[8],
                                                (const uint16_t*)d_in[9], (uint16_t*)d_out, (char*)d_ws);
}
```
